```python
import torch

def _softmax(x, axis=-1):
    return torch.softmax(x, dim=axis)
def _gelu(x):
    return torch.nn.functional.gelu(x, approximate="tanh")
def _silu(x):
    return torch.nn.functional.silu(x)
def _sigmoid(x):
    return torch.sigmoid(x)
def _layernorm(x, axis=-1, eps=1e-5):
    m = x.mean(dim=axis, keepdim=True)
    v = x.var(dim=axis, keepdim=True, unbiased=False)
    return (x - m) / torch.sqrt(v + eps)
def _scatter_add(tgt, idx, src, axis=0):
    return tgt.clone().index_add_(axis, idx, src)
def _segment_sum(src, seg, n):
    out = torch.zeros((n,) + src.shape[1:], dtype=src.dtype)
    return out.index_add_(0, seg, src)
def _pad(x, pw):
    flat = tuple(v for pair in reversed(pw) for v in pair)
    return torch.nn.functional.pad(x, flat)

kernel_name = 'sequential_preLN__mha_block_cached__swiglu_ffn__decode'

def setup_inputs():

    _w = (lambda a: (lambda t: t.to(torch.bfloat16) if t.is_floating_point() else t)(torch.as_tensor(a)))
    def _A_setup():
        B, Sq, D = 8, 1, 512
        H, Sk, Dh = 32, 4096, 128
        return {
            "x": _w((lambda *s: torch.randn(*s, dtype=torch.bfloat16) * 0.05)(B, Sq, D)),
            "k_cache": _w((lambda *s: torch.randn(*s, dtype=torch.bfloat16) * 3.2)(B, H, Sk, Dh)),
            "v_cache": _w((lambda *s: torch.randn(*s, dtype=torch.bfloat16) * 0.4)(B, H, Sk, Dh)),
            "wq": _w((lambda *s: torch.randn(*s, dtype=torch.bfloat16) * 3.2)(D, H * Dh) * (D ** -0.5)),
            "wo": _w((lambda *s: torch.randn(*s, dtype=torch.bfloat16) * 0.4)(H * Dh, D) * ((H * Dh) ** -0.5)),
        }
    _A_in = _A_setup()

    def _B_setup():
        B, S, D, F = 8, 1, 512, 1376
        return {
            "x": _w((lambda *s: torch.randn(*s, dtype=torch.bfloat16) * 0.05)(B, S, D)),
            "w_gate": _w((lambda *s: torch.randn(*s, dtype=torch.bfloat16) * 0.05)(D, F)),
            "w_up": _w((lambda *s: torch.randn(*s, dtype=torch.bfloat16) * 0.05)(D, F)),
            "w_down": _w((lambda *s: torch.randn(*s, dtype=torch.bfloat16) * 0.05)(F, D)),
        }
    _B_in = _B_setup()

    return {
        "A_x": _A_in["x"],
        "A_k_cache": _A_in["k_cache"],
        "A_v_cache": _A_in["v_cache"],
        "A_wq": _A_in["wq"],
        "A_wo": _A_in["wo"],
        "B_x": _B_in["x"],
        "B_w_gate": _B_in["w_gate"],
        "B_w_up": _B_in["w_up"],
        "B_w_down": _B_in["w_down"],
    }

def reference(A_x, A_k_cache, A_v_cache, A_wq, A_wo, B_x, B_w_gate, B_w_up, B_w_down):
    def _A_ref(x, k_cache, v_cache, wq, wo):
        B, Sq, D = x.shape
        H, Dh = 32, 128
        q = (lambda x, *d: x.permute(*d))((x @ wq).reshape(B, Sq, H, Dh), 0, 2, 1, 3)
        scores = torch.einsum("bhqd,bhkd->bhqk", q, k_cache) * (Dh ** -0.5)
        p = _softmax(scores, axis=-1)
        o = torch.einsum("bhqk,bhkd->bhqd", p, v_cache)
        return (lambda x, *d: x.permute(*d))(o, 0, 2, 1, 3).reshape(B, Sq, H * Dh) @ wo

    def _B_ref(x, w_gate, w_up, w_down):
        g = _silu(x @ w_gate)
        u = x @ w_up
        return (g * u) @ w_down

    x = A_x
    h = x + _A_ref(_layernorm(x, axis=-1), A_k_cache, A_v_cache, A_wq, A_wo)
    h2 = h + _B_ref(_layernorm(h, axis=-1), B_w_gate, B_w_up, B_w_down)
    return h2
```

```cpp
#include <hip/hip_runtime.h>
#include <hip/hip_bf16.h>
#include <stdint.h>

#define FUSED 0
#define KV_NT 1

namespace {

constexpr int NB = 8, DM = 512, NH = 32, SK = 4096, DH = 128, FF = 1376;
constexpr int NW = 16, NT = NW * 64;
constexpr unsigned SENT = 0xFFFFFFFFu;
constexpr unsigned SPIN_CAP = 400000u;

using u16 = unsigned short;
using u32x4 = __attribute__((ext_vector_type(4))) unsigned;
using u32x2 = __attribute__((ext_vector_type(2))) unsigned;
using f32x4 = __attribute__((ext_vector_type(4))) float;
using f32x2 = __attribute__((ext_vector_type(2))) float;
using bf16x2 = __attribute__((ext_vector_type(2))) __bf16;

constexpr size_t WS_PART = 0;
constexpr size_t PART_BYTES = (size_t)NB * NH * DM * 4;
constexpr int ACT_LD = 1408;
constexpr size_t WS_ACT = WS_PART + PART_BYTES;
constexpr size_t ACT_BYTES = (size_t)NB * ACT_LD * 4;
constexpr size_t WS_INIT_BYTES = PART_BYTES + ACT_BYTES;
constexpr size_t WS_H = WS_ACT + ACT_BYTES;

constexpr int L_GU = 0;
constexpr int L_P = L_GU + 16 * 8 * 768;
constexpr int L_QP = L_P + 8192;
constexpr int L_QF = L_QP + 8192;
constexpr int L_ST = L_QF + 256;
constexpr int L_OF = L_ST + 128;
constexpr int L_HS = L_OF + 256;
constexpr int L_HR = L_HS + 4096;
constexpr int L_GP = L_HR + 64;
constexpr int L_DP = L_GP + 16 * 96 * 4;
constexpr int L_END = L_DP + 1024;

template <typename T, typename F> __device__ __forceinline__ T bc(F x) { return __builtin_bit_cast(T, x); }
__device__ __forceinline__ float rbf(float x) { return (float)(__bf16)x; }
__device__ __forceinline__ unsigned pk2(float lo, float hi) { bf16x2 v = {(__bf16)lo, (__bf16)hi}; return bc<unsigned>(v); }
__device__ __forceinline__ float bflo(unsigned u) { return bc<float>(u << 16); }
__device__ __forceinline__ float bfhi(unsigned u) { return bc<float>(u & 0xffff0000u); }
__device__ __forceinline__ float dot2(unsigned a, unsigned b, float c) {
  return __builtin_amdgcn_fdot2_f32_bf16(bc<bf16x2>(a), bc<bf16x2>(b), c, false);
}
template <int CTRL> __device__ __forceinline__ float dpp(float x) {
  return bc<float>(__builtin_amdgcn_mov_dpp(bc<int>(x), CTRL, 0xf, 0xf, true));
}
constexpr int XOR1 = 0xB1, XOR2 = 0x4E, XOR7 = 0x141, ROR8 = 0x128, ROR4 = 0x124;
__device__ __forceinline__ float swapadd32(float a, float b) {
  auto s = __builtin_amdgcn_permlane32_swap(bc<unsigned>(a), bc<unsigned>(b), false, false);
  return bc<float>(s[0]) + bc<float>(s[1]);
}
__device__ __forceinline__ float swapadd16(float a, float b) {
  auto s = __builtin_amdgcn_permlane16_swap(bc<unsigned>(a), bc<unsigned>(b), false, false);
  return bc<float>(s[0]) + bc<float>(s[1]);
}
__device__ __forceinline__ float swapmax32(float a) {
  auto s = __builtin_amdgcn_permlane32_swap(bc<unsigned>(a), bc<unsigned>(a), false, false);
  return fmaxf(bc<float>(s[0]), bc<float>(s[1]));
}
__device__ __forceinline__ float swapmax16(float a) {
  auto s = __builtin_amdgcn_permlane16_swap(bc<unsigned>(a), bc<unsigned>(a), false, false);
  return fmaxf(bc<float>(s[0]), bc<float>(s[1]));
}
__device__ __forceinline__ float row_sum(float x) {
  x += dpp<XOR1>(x); x += dpp<XOR2>(x); x += dpp<XOR7>(x); x += dpp<ROR8>(x); return x;
}
__device__ __forceinline__ float row_max(float x) {
  x = fmaxf(x, dpp<XOR1>(x)); x = fmaxf(x, dpp<XOR2>(x)); x = fmaxf(x, dpp<XOR7>(x)); x = fmaxf(x, dpp<ROR8>(x)); return x;
}
__device__ __forceinline__ float wave_sum(float x) { x = row_sum(x); x = swapadd16(x, x); return swapadd32(x, x); }
__device__ __forceinline__ float wave_max(float x) { x = row_max(x); x = swapmax16(x); return swapmax32(x); }

__device__ __forceinline__ void mv2(const u32x4& w0, const u32x4& w1, unsigned xp, float (&acc)[8]) {
#pragma unroll
  for (int k = 0; k < 4; ++k) {
    const unsigned lo = __builtin_amdgcn_perm(w1[k], w0[k], 0x05040100u);
    const unsigned hi = __builtin_amdgcn_perm(w1[k], w0[k], 0x07060302u);
    acc[2 * k] = dot2(lo, xp, acc[2 * k]);
    acc[2 * k + 1] = dot2(hi, xp, acc[2 * k + 1]);
  }
}

__device__ __forceinline__ void ln_row(const float (&v)[8], unsigned (&o)[4]) {
  float s = 0.f;
#pragma unroll
  for (int k = 0; k < 8; ++k) s += v[k];
  const float mean = wave_sum(s) * (1.f / DM);
  float q = 0.f;
#pragma unroll
  for (int k = 0; k < 8; ++k) { const float d = v[k] - mean; q = fmaf(d, d, q); }
  const float var = wave_sum(q) * (1.f / DM);
  const float mb = rbf(mean);
  const float den = rbf(sqrtf(rbf(rbf(var) + 1e-5f)));
#pragma unroll
  for (int k = 0; k < 4; ++k) o[k] = pk2(rbf(v[2 * k] - mb) / den, rbf(v[2 * k + 1] - mb) / den);
}

template <int I>
__device__ __forceinline__ u32x4 ldkv(__amdgpu_buffer_rsrc_t rs, int voff, int soff) {
  return __builtin_amdgcn_raw_buffer_load_b128(rs, voff + (I & 3) * 1024, soff + (I >> 2) * 4096, KV_NT ? 2 : 0);
}
__device__ __forceinline__ int opaque(int v) { asm volatile("" : "+v"(v)); return v; }
#define SB() __builtin_amdgcn_sched_barrier(0)

__device__ __forceinline__ void st_sc1_b64(void* p, float a, float b) {
  unsigned ua = bc<unsigned>(a), ub = bc<unsigned>(b);
  ua = (ua == SENT) ? 0x7FC00000u : ua; ub = (ub == SENT) ? 0x7FC00000u : ub;
  __hip_atomic_store(reinterpret_cast<unsigned long long*>(p), ((unsigned long long)ub << 32) | ua, __ATOMIC_RELAXED, __HIP_MEMORY_SCOPE_AGENT);
}

__device__ __forceinline__ int chunk0(int j) { return 5 * j + (j < 12 ? j : 12); }

template <bool POLL>
__device__ __forceinline__ void tail1(unsigned char* lds, const u32x4 (&GW)[8], const u16* x, unsigned char* ws,
                                      int b, int j, int w, int lane) {
  const int r = lane >> 4, c = lane & 15;
  const int nch = j < 12 ? 6 : 5;
  const u32x4 xr = *reinterpret_cast<const u32x4*>(x + b * DM + lane * 8);
  const __amdgpu_buffer_rsrc_t rs = __builtin_amdgcn_make_buffer_rsrc((void*)(ws + WS_PART + (size_t)b * NH * DM * 4), 0, NH * DM * 4, 0x00020000);
  const int off0 = ((16 * (w >> 3) + r) * DM + 64 * (w & 7) + 4 * c) * 4;
  u32x4 v[4];
  if constexpr (POLL) {
    bool done[4] = {false, false, false, false};
    for (unsigned spin = 0;; ++spin) {
      asm volatile("" ::: "memory");
#pragma unroll
      for (int i = 0; i < 4; ++i) if (!done[i]) v[i] = __builtin_amdgcn_raw_buffer_load_b128(rs, off0 + i * 4 * DM * 4, 0, 16);
#pragma unroll
      for (int i = 0; i < 4; ++i) if (!done[i]) done[i] = __all(v[i][0] != SENT && v[i][1] != SENT && v[i][2] != SENT && v[i][3] != SENT);
      if ((done[0] && done[1] && done[2] && done[3]) || spin > SPIN_CAP) break;
      __builtin_amdgcn_s_sleep(2);
    }
  } else {
#pragma unroll
    for (int i = 0; i < 4; ++i) v[i] = __builtin_amdgcn_raw_buffer_load_b128(rs, off0 + i * 4 * DM * 4, 0, 0);
  }
  float s[4];
#pragma unroll
  for (int k = 0; k < 4; ++k) s[k] = ((bc<float>(v[0][k]) + bc<float>(v[1][k])) + bc<float>(v[2][k])) + bc<float>(v[3][k]);
  {
    const float y0 = swapadd32(s[0], s[2]), y1 = swapadd32(s[1], s[3]);
    const float z = swapadd16(y0, y1);
    reinterpret_cast<float*>(lds + L_HS)[(w >> 3) * DM + 64 * (w & 7) + 4 * c + 2 * (lane >> 5) + (r & 1)] = z;
  }
  __syncthreads();
  float hv[8];
  {
    const f32x4* hs = reinterpret_cast<const f32x4*>(lds + L_HS);
    const f32x4 a0 = hs[2 * lane], a1 = hs[2 * lane + 1], b0 = hs[128 + 2 * lane], b1 = hs[128 + 2 * lane + 1];
#pragma unroll
    for (int k = 0; k < 4; ++k) {
      const float xa = (k & 1) ? bfhi(xr[k >> 1]) : bflo(xr[k >> 1]);
      const float xb = (k & 1) ? bfhi(xr[2 + (k >> 1)]) : bflo(xr[2 + (k >> 1)]);
      hv[k] = rbf(xa + rbf(a0[k] + b0[k]));
      hv[4 + k] = rbf(xb + rbf(a1[k] + b1[k]));
    }
  }
  if (w == 0 && (lane >> 1) == j) {
    float* hr = reinterpret_cast<float*>(lds + L_HR) + (lane & 1) * 8;
#pragma unroll
    for (int k = 0; k < 8; ++k) hr[k] = hv[k];
    if constexpr (!POLL) {
      float* hg = reinterpret_cast<float*>(ws + WS_H) + b * DM + lane * 8;
#pragma unroll
      for (int k = 0; k < 8; ++k) hg[k] = hv[k];
    }
  }
  unsigned hp[4];
  ln_row(hv, hp);
  float acc[8] = {0.f, 0.f, 0.f, 0.f, 0.f, 0.f, 0.f, 0.f};
#pragma unroll
  for (int e = 0; e < 4; ++e) {
    const unsigned xp = (unsigned)__shfl((int)hp[e], 4 * w + r);
    mv2(GW[2 * e], GW[2 * e + 1], xp, acc);
  }
  {
    float y[4];
#pragma unroll
    for (int k = 0; k < 4; ++k) y[k] = swapadd32(acc[k], acc[k + 4]);
    const float z0 = swapadd16(y[0], y[2]), z1 = swapadd16(y[1], y[3]);
    if (c < 2 * nch) *reinterpret_cast<f32x2*>(lds + L_GP + (w * 96 + c * 8 + 4 * (lane >> 5) + 2 * (r & 1)) * 4) = f32x2{z0, z1};
  }
  __syncthreads();
  if (w == 0 && lane < 8 * nch) {
    const float* gp = reinterpret_cast<const float*>(lds + L_GP);
    float g = 0.f, u = 0.f;
#pragma unroll
    for (int k = 0; k < NW; ++k) { g += gp[k * 96 + lane]; u += gp[k * 96 + 8 * nch + lane]; }
    g = rbf(g); u = rbf(u);
    const float sg = rbf(g / (1.f + expf(-g)));
    const float a = rbf(sg * u);
    unsigned* ap = reinterpret_cast<unsigned*>(ws + WS_ACT) + b * ACT_LD + chunk0(j) * 8 + lane;
    __hip_atomic_store(ap, bc<unsigned>(a) & 0xffff0000u, __ATOMIC_RELAXED, __HIP_MEMORY_SCOPE_AGENT);
  }
}

template <bool POLL>
__device__ __forceinline__ void tail2(unsigned char* lds, const u32x4 (&DW)[3], unsigned char* ws, u16* out,
                                      int b, int j, int w, int lane) {
  const int tid = w * 64 + lane;
  const bool has3 = w < 11;
  const unsigned* ap = reinterpret_cast<const unsigned*>(ws + WS_ACT) + b * ACT_LD + (tid >> 1);
  unsigned a[3];
  if constexpr (POLL) {
    for (unsigned spin = 0;; ++spin) {
      a[0] = __hip_atomic_load(ap, __ATOMIC_RELAXED, __HIP_MEMORY_SCOPE_AGENT);
      a[1] = __hip_atomic_load(ap + 512, __ATOMIC_RELAXED, __HIP_MEMORY_SCOPE_AGENT);
      a[2] = has3 ? __hip_atomic_load(ap + 1024, __ATOMIC_RELAXED, __HIP_MEMORY_SCOPE_AGENT) : 0u;
      if (__all(a[0] != SENT && a[1] != SENT && a[2] != SENT) || spin > SPIN_CAP) break;
      __builtin_amdgcn_s_sleep(1);
    }
  } else {
    a[0] = ap[0]; a[1] = ap[512]; a[2] = has3 ? ap[1024] : 0u;
  }
  float acc[8];
#pragma unroll
  for (int k = 0; k < 4; ++k) {
    acc[2 * k] = bc<float>(a[0]) * bflo(DW[0][k]); acc[2 * k + 1] = bc<float>(a[0]) * bfhi(DW[0][k]);
#pragma unroll
    for (int i = 1; i < 3; ++i) {
      acc[2 * k] = fmaf(bc<float>(a[i]), bflo(DW[i][k]), acc[2 * k]);
      acc[2 * k + 1] = fmaf(bc<float>(a[i]), bfhi(DW[i][k]), acc[2 * k + 1]);
    }
  }
  float y[4];
#pragma unroll
  for (int k = 0; k < 4; ++k) y[k] = swapadd32(acc[k], acc[k + 4]);
  float z0 = swapadd16(y[0], y[2]), z1 = swapadd16(y[1], y[3]);
  z0 += dpp<ROR8>(z0); z1 += dpp<ROR8>(z1);
  z0 += dpp<ROR4>(z0); z1 += dpp<ROR4>(z1);
  z0 += dpp<XOR2>(z0); z1 += dpp<XOR2>(z1);
  if ((lane & 14) == 0)
    *reinterpret_cast<f32x2*>(lds + L_DP + (w * 16 + 8 * (lane & 1) + 4 * (lane >> 5) + 2 * ((lane >> 4) & 1)) * 4) = f32x2{z0, z1};
  __syncthreads();
  if (tid < 16) {
    const float* dp = reinterpret_cast<const float*>(lds + L_DP);
    float s = 0.f;
#pragma unroll
    for (int k = 0; k < NW; ++k) s += dp[k * 16 + tid];
    float hres;
    if constexpr (POLL) hres = reinterpret_cast<const float*>(lds + L_HR)[tid];
    else hres = reinterpret_cast<const float*>(ws + WS_H)[b * DM + 16 * j + tid];
    const __bf16 o = (__bf16)(hres + rbf(s));
    out[b * DM + 16 * j + tid] = bc<u16>(o);
  }
}

__device__ __forceinline__ void load_gu(u32x4 (&A)[8], const u16* wg, const u16* wu, int j, int w, int lane) {
  const int nch = j < 12 ? 6 : 5;
  int cm = lane & 15; cm = cm < 2 * nch ? cm : 2 * nch - 1;
  const bool up = cm >= nch;
  const u16* base = (up ? wu : wg) + (size_t)(32 * w + 8 * (lane >> 4)) * FF + (chunk0(j) + (up ? cm - nch : cm)) * 8;
#pragma unroll
  for (int e = 0; e < 8; ++e) A[e] = *reinterpret_cast<const u32x4*>(base + e * FF);
}
__device__ __forceinline__ void load_dw(u32x4 (&DW)[3], const u16* wd, int j, int w, int lane) {
  const int tid = w * 64 + lane;
  const u16* base = wd + (size_t)(tid >> 1) * DM + 16 * j + 8 * (tid & 1);
  DW[0] = *reinterpret_cast<const u32x4*>(base);
  DW[1] = *reinterpret_cast<const u32x4*>(base + 512 * DM);
  if (w < 11) DW[2] = *reinterpret_cast<const u32x4*>(base + 1024 * DM);
  else DW[2] = u32x4{0u, 0u, 0u, 0u};
}

template <bool FUSE>
__global__ void __launch_bounds__(NT, 4)
attn_kernel(const u16* __restrict__ x, const u16* __restrict__ kc, const u16* __restrict__ vc, const u16* __restrict__ wq,
            const u16* __restrict__ wo, const u16* __restrict__ wg, const u16* __restrict__ wu, const u16* __restrict__ wd,
            u16* __restrict__ out, unsigned char* __restrict__ ws) {
  __shared__ __attribute__((aligned(16))) unsigned char lds[L_END];
  const int tid = threadIdx.x, lane = tid & 63;
  const int w = __builtin_amdgcn_readfirstlane(tid >> 6);
  const int r = lane >> 4, c = lane & 15;
  const int bid = blockIdx.x;
  const int h = (bid & 7) * 4 + ((bid >> 3) & 3), b = bid >> 5;
  const int rot = (bid >> 3) + (bid >> 6);

  const __amdgpu_buffer_rsrc_t rK = __builtin_amdgcn_make_buffer_rsrc((void*)(kc + (size_t)(b * NH + h) * SK * DH), 0, SK * DH * 2, 0x00020000);
  const __amdgpu_buffer_rsrc_t rV = __builtin_amdgcn_make_buffer_rsrc((void*)(vc + (size_t)(b * NH + h) * SK * DH), 0, SK * DH * 2, 0x00020000);
  const int voff = w * 8192 + lane * 16;
#define GOFF(g) ((((g) + rot) & 7) * (16 * 8192))

  const u32x4 xr = *reinterpret_cast<const u32x4*>(x + b * DM + lane * 8);
  u32x4 A[8], R[8], DW[3];
  {
    const u16* base = wq + (size_t)(32 * w + 8 * r) * (NH * DH) + h * DH + 8 * c;
#pragma unroll
    for (int e = 0; e < 8; ++e) A[e] = *reinterpret_cast<const u32x4*>(base + e * (NH * DH));
  }
  SB();
  R[0] = ldkv<0>(rK, voff, GOFF(0)); R[1] = ldkv<1>(rK, voff, GOFF(0)); R[2] = ldkv<2>(rK, voff, GOFF(0)); R[3] = ldkv<3>(rK, voff, GOFF(0));
  R[4] = ldkv<4>(rK, voff, GOFF(0)); R[5] = ldkv<5>(rK, voff, GOFF(0)); R[6] = ldkv<6>(rK, voff, GOFF(0)); R[7] = ldkv<7>(rK, voff, GOFF(0));
  SB();
  if constexpr (FUSE) load_dw(DW, wd, h, w, lane);

  {
    float xv[8];
#pragma unroll
    for (int k = 0; k < 4; ++k) { xv[2 * k] = bflo(xr[k]); xv[2 * k + 1] = bfhi(xr[k]); }
    unsigned xn[4];
    ln_row(xv, xn);
    float acc[8] = {0.f, 0.f, 0.f, 0.f, 0.f, 0.f, 0.f, 0.f};
#pragma unroll
    for (int e = 0; e < 4; ++e) {
      const unsigned xp = (unsigned)__shfl((int)xn[e], 4 * w + r);
      mv2(A[2 * e], A[2 * e + 1], xp, acc);
    }
    float y[4];
#pragma unroll
    for (int k = 0; k < 4; ++k) y[k] = swapadd32(acc[k], acc[k + 4]);
    const float z0 = swapadd16(y[0], y[2]), z1 = swapadd16(y[1], y[3]);
    *reinterpret_cast<f32x2*>(lds + L_QP + (w * DH + 8 * c + 4 * (lane >> 5) + 2 * (r & 1)) * 4) = f32x2{z0, z1};
  }
  if constexpr (FUSE) load_gu(A, wg, wu, h, w, lane);
  __syncthreads();
  if (tid < 64) {
    const f32x2* qp = reinterpret_cast<const f32x2*>(lds + L_QP);
    float a0 = 0.f, a1 = 0.f;
#pragma unroll
    for (int k = 0; k < NW; ++k) { const f32x2 t = qp[k * 64 + tid]; a0 += t[0]; a1 += t[1]; }
    reinterpret_cast<unsigned*>(lds + L_QF)[tid] = pk2(a0, a1);
  }
  __syncthreads();
  const u32x4 q = *reinterpret_cast<const u32x4*>(lds + L_QF + c * 16);

  float sc[8];
#pragma unroll
  for (int g = 0; g < 8; ++g) {
    float xs[8];
    const __amdgpu_buffer_rsrc_t rn = (g < 7) ? rK : rV;
    const int so = (g < 7) ? GOFF(g + 1) : GOFF(0);
#define KSTEP(i) { float d = dot2(R[i][0], q[0], 0.f); d = dot2(R[i][1], q[1], d); d = dot2(R[i][2], q[2], d); d = dot2(R[i][3], q[3], d); \
      xs[i] = d; SB(); R[i] = ldkv<i>(rn, voff, so); SB(); }
    KSTEP(0) KSTEP(1) KSTEP(2) KSTEP(3) KSTEP(4) KSTEP(5) KSTEP(6) KSTEP(7)
#undef KSTEP
#pragma unroll
    for (int k = 0; k < 4; ++k) {
      const float t1 = xs[k] + dpp<ROR8>(xs[k]), t2 = xs[k + 4] + dpp<ROR8>(xs[k + 4]);
      xs[k] = (c & 8) ? t2 : t1;
    }
#pragma unroll
    for (int k = 0; k < 2; ++k) {
      const float t1 = xs[k] + dpp<XOR7>(xs[k]), t2 = xs[k + 2] + dpp<XOR7>(xs[k + 2]);
      xs[k] = (c & 4) ? t2 : t1;
    }
    {
      const float t1 = xs[0] + dpp<XOR2>(xs[0]), t2 = xs[1] + dpp<XOR2>(xs[1]);
      float s = (c & 2) ? t2 : t1;
      s += dpp<XOR1>(s);
      sc[g] = rbf(rbf(s) * 0.08838834764831845f);
    }
    if (g == 1) {
      const int l1 = opaque(lane);
      if (FUSE && (l1 & 15) < 12) {
#pragma unroll
        for (int e = 0; e < 8; ++e) *reinterpret_cast<u32x4*>(lds + L_GU + ((w * 8 + e) * 48 + (l1 >> 4) * 12 + (l1 & 15)) * 16) = A[e];
      }
      SB();
      const u16* base = wo + (size_t)(h * DH + (l1 >> 2)) * DM + 32 * w + 8 * (l1 & 3);
#pragma unroll
      for (int i = 0; i < 8; ++i) A[i] = *reinterpret_cast<const u32x4*>(base + i * 16 * DM);
      SB();
    }
  }

  {
    float m = sc[0];
#pragma unroll
    for (int g = 1; g < 8; ++g) m = fmaxf(m, sc[g]);
    m = wave_max(m);
    float l = 0.f;
#pragma unroll
    for (int g = 0; g < 8; ++g) l += expf(sc[g] - m);
    l = wave_sum(l) * 0.5f;
    if (lane == 0) *reinterpret_cast<f32x2*>(lds + L_ST + w * 8) = f32x2{m, l};
  }
  __syncthreads();
  {
    const f32x2 st = *reinterpret_cast<const f32x2*>(lds + L_ST + c * 8);
    const float M = row_max(st[0]);
    const float L = row_sum(st[1] * expf(st[0] - M));
    if ((c & 1) == 0) {
#pragma unroll
      for (int g = 0; g < 8; ++g) {
        const __bf16 p = (__bf16)(expf(sc[g] - M) / L);
        *reinterpret_cast<u16*>(lds + L_P + ((w * 8 + g) * 4 + r) * 16 + (c >> 1) * 2) = bc<u16>(p);
      }
    }
  }

  float acc[8] = {0.f, 0.f, 0.f, 0.f, 0.f, 0.f, 0.f, 0.f};
#pragma unroll
  for (int g = 0; g < 8; ++g) {
    const u32x4 pw = *reinterpret_cast<const u32x4*>(lds + L_P + ((w * 8 + g) * 4 + r) * 16);
    const int so = GOFF(g + 1);
#define VSTEP(ip) { mv2(R[2 * ip], R[2 * ip + 1], pw[ip], acc); SB(); \
      if (g < 7) { R[2 * ip] = ldkv<2 * ip>(rV, voff, so); R[2 * ip + 1] = ldkv<2 * ip + 1>(rV, voff, so); } SB(); }
    VSTEP(0) VSTEP(1) VSTEP(2) VSTEP(3)
#undef VSTEP
  }
  const int ln2 = opaque(lane), r2 = ln2 >> 4, c2 = ln2 & 15, td2 = w * 64 + ln2;
  {
    float y[4];
#pragma unroll
    for (int k = 0; k < 4; ++k) y[k] = swapadd32(acc[k], acc[k + 4]);
    const float z0 = swapadd16(y[0], y[2]), z1 = swapadd16(y[1], y[3]);
    *reinterpret_cast<f32x2*>(lds + L_QP + (w * DH + 8 * c2 + 4 * (ln2 >> 5) + 2 * (r2 & 1)) * 4) = f32x2{z0, z1};
  }
  __syncthreads();
  if (td2 < DH) {
    const float* op = reinterpret_cast<const float*>(lds + L_QP);
    float s = 0.f;
#pragma unroll
    for (int k = 0; k < NW; ++k) s += op[k * DH + td2];
    const __bf16 o = (__bf16)s;
    *reinterpret_cast<u16*>(lds + L_OF + ((td2 & 15) * 8 + (td2 >> 4)) * 2) = bc<u16>(o);
  }
  __syncthreads();
  {
    const u32x4 ov = *reinterpret_cast<const u32x4*>(lds + L_OF + (ln2 >> 2) * 16);
    float pa[8] = {0.f, 0.f, 0.f, 0.f, 0.f, 0.f, 0.f, 0.f};
#pragma unroll
    for (int e = 0; e < 4; ++e) mv2(A[2 * e], A[2 * e + 1], ov[e], pa);
    float y[4];
#pragma unroll
    for (int k = 0; k < 4; ++k) y[k] = swapadd32(pa[k], pa[k + 4]);
    float z0 = swapadd16(y[0], y[2]), z1 = swapadd16(y[1], y[3]);
    z0 += dpp<ROR8>(z0); z1 += dpp<ROR8>(z1);
    z0 += dpp<ROR4>(z0); z1 += dpp<ROR4>(z1);
    if ((ln2 & 12) == 0)
      st_sc1_b64(ws + WS_PART + ((size_t)(b * NH + h) * DM + 32 * w + 8 * (ln2 & 3) + 4 * (ln2 >> 5) + 2 * (r2 & 1)) * 4, z0, z1);
  }
  if constexpr (FUSE) {
    const int cm = c2 < 12 ? c2 : 11;
#pragma unroll
    for (int e = 0; e < 8; ++e) R[e] = *reinterpret_cast<const u32x4*>(lds + L_GU + ((w * 8 + e) * 48 + r2 * 12 + cm) * 16);
    tail1<true>(lds, R, x, ws, b, h, w, ln2);
    tail2<true>(lds, DW, ws, out, b, h, w, ln2);
  }
#undef GOFF
}

__global__ void __launch_bounds__(NT, 4)
ffn1_kernel(const u16* __restrict__ x, const u16* __restrict__ wg, const u16* __restrict__ wu, unsigned char* __restrict__ ws) {
  __shared__ __attribute__((aligned(16))) unsigned char lds[L_END];
  const int tid = threadIdx.x, lane = tid & 63;
  const int w = __builtin_amdgcn_readfirstlane(tid >> 6);
  const int bid = blockIdx.x;
  const int j = (bid & 7) * 4 + ((bid >> 3) & 3), b = bid >> 5;
  u32x4 A[8];
  load_gu(A, wg, wu, j, w, lane);
  tail1<false>(lds, A, x, ws, b, j, w, lane);
}

__global__ void __launch_bounds__(NT, 4)
ffn2_kernel(const u16* __restrict__ wd, u16* __restrict__ out, unsigned char* __restrict__ ws) {
  __shared__ __attribute__((aligned(16))) unsigned char lds[L_END];
  const int tid = threadIdx.x, lane = tid & 63;
  const int w = __builtin_amdgcn_readfirstlane(tid >> 6);
  const int bid = blockIdx.x;
  const int j = (bid & 7) * 4 + ((bid >> 3) & 3), b = bid >> 5;
  u32x4 DW[3];
  load_dw(DW, wd, j, w, lane);
  tail2<false>(lds, DW, ws, out, b, j, w, lane);
}

}

extern "C" void kernel_launch(void* const* d_in, const int* in_sizes, int n_in,
                              void* d_out, int out_size, void* d_ws, size_t ws_size,
                              hipStream_t stream) {
  const u16* x = (const u16*)d_in[0];
  const u16* kc = (const u16*)d_in[1];
  const u16* vc = (const u16*)d_in[2];
  const u16* wq = (const u16*)d_in[3];
  const u16* wo = (const u16*)d_in[4];
  const u16* wg = (const u16*)d_in[6];
  const u16* wu = (const u16*)d_in[7];
  const u16* wd = (const u16*)d_in[8];
  u16* out = (u16*)d_out;
  unsigned char* ws = (unsigned char*)d_ws;
  hipLaunchKernelGGL(attn_kernel<false>, dim3(NB * NH), dim3(NT), 0, stream, x, kc, vc, wq, wo, wg, wu, wd, out, ws);
  hipLaunchKernelGGL(ffn1_kernel, dim3(NB * NH), dim3(NT), 0, stream, x, wg, wu, ws);
  hipLaunchKernelGGL(ffn2_kernel, dim3(NB * NH), dim3(NT), 0, stream, wd, out, ws);
}
```

```cpp
#include <hip/hip_runtime.h>
#include <hip/hip_bf16.h>
#include <stdint.h>

#define KV_NT 1

#define STAMP(k)
#define TSARG
#define TSPASS
#define TSDECL
#define TSFLUSH

namespace {

constexpr int NB = 8, DM = 512, NH = 32, SK = 4096, DH = 128, FF = 1376;
constexpr int NW = 8, NT = NW * 64;
constexpr unsigned SENT = 0xFFFFFFFFu;
constexpr unsigned SPIN_CAP = 400000u;

using u16 = unsigned short;
using u32x4 = __attribute__((ext_vector_type(4))) unsigned;
using u32x2 = __attribute__((ext_vector_type(2))) unsigned;
using f32x4 = __attribute__((ext_vector_type(4))) float;
using f32x2 = __attribute__((ext_vector_type(2))) float;
using bf16x2 = __attribute__((ext_vector_type(2))) __bf16;

constexpr size_t WS_PART = 0;
constexpr size_t PART_BYTES = (size_t)NB * NH * DM * 4;
constexpr int ACT_LD = 1408;
constexpr size_t WS_ACT = WS_PART + PART_BYTES;
constexpr size_t ACT_BYTES = (size_t)NB * ACT_LD * 4;
constexpr size_t WS_INIT_BYTES = PART_BYTES + ACT_BYTES;
constexpr size_t WS_H = WS_ACT + ACT_BYTES;

constexpr int L_P = 0;
constexpr int L_QP = L_P + 8192;
constexpr int L_QF = L_QP + 4096;
constexpr int L_ST = L_QF + 256;
constexpr int L_OF = L_ST + 128;
constexpr int L_HS = L_OF + 256;
constexpr int L_HR = L_HS + 2048;
constexpr int L_GP = L_HR + 64;
constexpr int L_DP = L_GP + 8 * 96 * 4;
constexpr int L_END = L_DP + 512;

template <typename T, typename F> __device__ __forceinline__ T bc(F x) { return __builtin_bit_cast(T, x); }
__device__ __forceinline__ float rbf(float x) { return (float)(__bf16)x; }
__device__ __forceinline__ unsigned pk2(float lo, float hi) { bf16x2 v = {(__bf16)lo, (__bf16)hi}; return bc<unsigned>(v); }
__device__ __forceinline__ float bflo(unsigned u) { return bc<float>(u << 16); }
__device__ __forceinline__ float bfhi(unsigned u) { return bc<float>(u & 0xffff0000u); }
__device__ __forceinline__ float dot2(unsigned a, unsigned b, float c) {
  return __builtin_amdgcn_fdot2_f32_bf16(bc<bf16x2>(a), bc<bf16x2>(b), c, false);
}
template <int CTRL> __device__ __forceinline__ float dpp(float x) {
  return bc<float>(__builtin_amdgcn_mov_dpp(bc<int>(x), CTRL, 0xf, 0xf, true));
}
constexpr int XOR1 = 0xB1, XOR2 = 0x4E, XOR7 = 0x141, ROR8 = 0x128, ROR4 = 0x124;
__device__ __forceinline__ float swapadd32(float a, float b) {
  auto s = __builtin_amdgcn_permlane32_swap(bc<unsigned>(a), bc<unsigned>(b), false, false);
  return bc<float>(s[0]) + bc<float>(s[1]);
}
__device__ __forceinline__ float swapadd16(float a, float b) {
  auto s = __builtin_amdgcn_permlane16_swap(bc<unsigned>(a), bc<unsigned>(b), false, false);
  return bc<float>(s[0]) + bc<float>(s[1]);
}
__device__ __forceinline__ float swapmax32(float a) {
  auto s = __builtin_amdgcn_permlane32_swap(bc<unsigned>(a), bc<unsigned>(a), false, false);
  return fmaxf(bc<float>(s[0]), bc<float>(s[1]));
}
__device__ __forceinline__ float swapmax16(float a) {
  auto s = __builtin_amdgcn_permlane16_swap(bc<unsigned>(a), bc<unsigned>(a), false, false);
  return fmaxf(bc<float>(s[0]), bc<float>(s[1]));
}
__device__ __forceinline__ float row_sum(float x) {
  x += dpp<XOR1>(x); x += dpp<XOR2>(x); x += dpp<XOR7>(x); x += dpp<ROR8>(x); return x;
}
__device__ __forceinline__ float row_max(float x) {
  x = fmaxf(x, dpp<XOR1>(x)); x = fmaxf(x, dpp<XOR2>(x)); x = fmaxf(x, dpp<XOR7>(x)); x = fmaxf(x, dpp<ROR8>(x)); return x;
}
__device__ __forceinline__ float wave_sum(float x) { x = row_sum(x); x = swapadd16(x, x); return swapadd32(x, x); }
__device__ __forceinline__ float wave_max(float x) { x = row_max(x); x = swapmax16(x); return swapmax32(x); }

__device__ __forceinline__ void mv2(const u32x4& w0, const u32x4& w1, unsigned xp, float (&acc)[8]) {
#pragma unroll
  for (int k = 0; k < 4; ++k) {
    const unsigned lo = __builtin_amdgcn_perm(w1[k], w0[k], 0x05040100u);
    const unsigned hi = __builtin_amdgcn_perm(w1[k], w0[k], 0x07060302u);
    acc[2 * k] = dot2(lo, xp, acc[2 * k]);
    acc[2 * k + 1] = dot2(hi, xp, acc[2 * k + 1]);
  }
}

__device__ __forceinline__ void ln_row(const float (&v)[8], unsigned (&o)[4]) {
  float s = 0.f;
#pragma unroll
  for (int k = 0; k < 8; ++k) s += v[k];
  const float mean = wave_sum(s) * (1.f / DM);
  float q = 0.f;
#pragma unroll
  for (int k = 0; k < 8; ++k) { const float d = v[k] - mean; q = fmaf(d, d, q); }
  const float var = wave_sum(q) * (1.f / DM);
  const float mb = rbf(mean);
  const float den = rbf(sqrtf(rbf(rbf(var) + 1e-5f)));
#pragma unroll
  for (int k = 0; k < 4; ++k) o[k] = pk2(rbf(v[2 * k] - mb) / den, rbf(v[2 * k + 1] - mb) / den);
}

template <int I>
__device__ __forceinline__ u32x4 ldkv(__amdgpu_buffer_rsrc_t rs, int voff, int soff) {
  return __builtin_amdgcn_raw_buffer_load_b128(rs, voff + (I & 3) * 1024, soff + (I >> 2) * 4096, KV_NT ? 2 : 0);
}
__device__ __forceinline__ int opaque(int v) { asm volatile("" : "+v"(v)); return v; }
#define SB() __builtin_amdgcn_sched_barrier(0)

__device__ __forceinline__ void st_sc1_b64(void* p, float a, float b) {
  unsigned ua = bc<unsigned>(a), ub = bc<unsigned>(b);
  ua = (ua == SENT) ? 0x7FC00000u : ua; ub = (ub == SENT) ? 0x7FC00000u : ub;
  __hip_atomic_store(reinterpret_cast<unsigned long long*>(p), ((unsigned long long)ub << 32) | ua, __ATOMIC_RELAXED, __HIP_MEMORY_SCOPE_AGENT);
}

__device__ __forceinline__ int chunk0(int j) { return 5 * j + (j < 12 ? j : 12); }

__global__ void __launch_bounds__(NT, 2)
fused_kernel(const u16* __restrict__ x, const u16* __restrict__ kc, const u16* __restrict__ vc, const u16* __restrict__ wq,
             const u16* __restrict__ wo, const u16* __restrict__ wg, const u16* __restrict__ wu, const u16* __restrict__ wd,
             u16* __restrict__ out, unsigned char* __restrict__ ws) {
  __shared__ __attribute__((aligned(16))) unsigned char lds[L_END];
  const int tid = threadIdx.x, lane = tid & 63;
  const int w = __builtin_amdgcn_readfirstlane(tid >> 6);
  const int r = lane >> 4, c = lane & 15;
  const int bid = blockIdx.x;
  const int h = (bid & 7) * 4 + ((bid >> 3) & 3), b = bid >> 5;
  const int rot = (bid >> 3) + (bid >> 7);
  const int nch = h < 12 ? 6 : 5;

  const __amdgpu_buffer_rsrc_t rK = __builtin_amdgcn_make_buffer_rsrc((void*)(kc + (size_t)(b * NH + h) * SK * DH), 0, SK * DH * 2, 0x00020000);
  const __amdgpu_buffer_rsrc_t rV = __builtin_amdgcn_make_buffer_rsrc((void*)(vc + (size_t)(b * NH + h) * SK * DH), 0, SK * DH * 2, 0x00020000);
  const int voff = w * 8192 + lane * 16;
#define GOFF(g) ((((g) + rot) & 15) * (NW * 8192))

  TSDECL
  STAMP(0);
  const u32x4 xr = *reinterpret_cast<const u32x4*>(x + b * DM + lane * 8);
  u32x4 A[16], X[16], R[8], DW[6];
  {
    const u16* base = wq + (size_t)(64 * w + 16 * r) * (NH * DH) + h * DH + 8 * c;
#pragma unroll
    for (int e = 0; e < 16; ++e) A[e] = *reinterpret_cast<const u32x4*>(base + e * (NH * DH));
  }
  SB();
#define LD8(dst, o, rs, so) dst[o + 0] = ldkv<0>(rs, voff, so); dst[o + 1] = ldkv<1>(rs, voff, so); dst[o + 2] = ldkv<2>(rs, voff, so); dst[o + 3] = ldkv<3>(rs, voff, so); \
                            dst[o + 4] = ldkv<4>(rs, voff, so); dst[o + 5] = ldkv<5>(rs, voff, so); dst[o + 6] = ldkv<6>(rs, voff, so); dst[o + 7] = ldkv<7>(rs, voff, so);
  LD8(R, 0, rK, GOFF(0)) LD8(X, 0, rK, GOFF(1)) LD8(X, 8, rK, GOFF(2))
#undef LD8
  SB();

  {
    float xv[8];
#pragma unroll
    for (int k = 0; k < 4; ++k) { xv[2 * k] = bflo(xr[k]); xv[2 * k + 1] = bfhi(xr[k]); }
    unsigned xn[4];
    ln_row(xv, xn);
    float acc[8] = {0.f, 0.f, 0.f, 0.f, 0.f, 0.f, 0.f, 0.f};
#pragma unroll
    for (int e = 0; e < 8; ++e) {
      const unsigned xp = (unsigned)__shfl((int)xn[e & 3], 8 * w + 2 * r + (e >> 2));
      mv2(A[2 * e], A[2 * e + 1], xp, acc);
    }
    float y[4];
#pragma unroll
    for (int k = 0; k < 4; ++k) y[k] = swapadd32(acc[k], acc[k + 4]);
    const float z0 = swapadd16(y[0], y[2]), z1 = swapadd16(y[1], y[3]);
    *reinterpret_cast<f32x2*>(lds + L_QP + (w * DH + 8 * c + 4 * (lane >> 5) + 2 * (r & 1)) * 4) = f32x2{z0, z1};
  }
  __syncthreads();
  if (tid < 64) {
    const f32x2* qp = reinterpret_cast<const f32x2*>(lds + L_QP);
    float a0 = 0.f, a1 = 0.f;
#pragma unroll
    for (int k = 0; k < NW; ++k) { const f32x2 t = qp[k * 64 + tid]; a0 += t[0]; a1 += t[1]; }
    reinterpret_cast<unsigned*>(lds + L_QF)[tid] = pk2(a0, a1);
  }
  __syncthreads();
  const u32x4 q = *reinterpret_cast<const u32x4*>(lds + L_QF + c * 16);

  STAMP(1);
  const u16* gub;
  {
    int cm = c < 2 * nch ? c : 2 * nch - 1;
    const bool up = cm >= nch;
    gub = (up ? wu : wg) + (size_t)(64 * w + 16 * r) * FF + (chunk0(h) + (up ? cm - nch : cm)) * 8;
  }
  float sc[16];
#pragma unroll
  for (int g = 0; g < 16; ++g) {
    float xs[8];
    const int sc0 = GOFF(g);
    const __amdgpu_buffer_rsrc_t rn = (g < 15) ? rK : rV;
    const int sn = (g < 15) ? GOFF(g + 1) : GOFF(0);
    const int l1 = opaque(lane);
    const u16* wob = wo + (size_t)(h * DH + (l1 >> 3)) * DM + 64 * w + 8 * (l1 & 7);
    const u16* wdb = wd + (size_t)((w * 64 + l1) >> 1) * DM + 16 * h + 8 * (l1 & 1);
#define KDOT(src) { float d = dot2(src[0], q[0], 0.f); d = dot2(src[1], q[1], d); d = dot2(src[2], q[2], d); d = dot2(src[3], q[3], d); xs[i_] = d; }
#define KSTEP(i) { constexpr int i_ = i; \
      if (g == 1) { KDOT(X[i]) SB(); X[i] = *reinterpret_cast<const u32x4*>(gub + (i) * FF); } \
      else if (g == 2) { KDOT(X[8 + i]) SB(); X[8 + i] = *reinterpret_cast<const u32x4*>(gub + (8 + i) * FF); } \
      else { KDOT(R[i]) SB(); \
        if (g == 0) R[i] = ldkv<i>(rK, voff, GOFF(3)); \
        else if (i < 4) { if (g > 3) R[(i + 4) & 7] = ldkv<(i + 4) & 7>(rK, voff, sc0); } \
        else R[(i + 4) & 7] = ldkv<(i + 4) & 7>(rn, voff, sn); \
        if (g >= 4 && g < 12 && (i & 3) == 1) A[2 * (g - 4) + (i >> 2)] = *reinterpret_cast<const u32x4*>(wob + (2 * (g - 4) + (i >> 2)) * 8 * DM); \
        if (g >= 12 && g < 15 && (i & 3) == 1) { constexpr int dd = (i >> 2); const int di = 2 * (g - 12) + dd; \
          if (di < 5 || w < 3) DW[di] = *reinterpret_cast<const u32x4*>(wdb + di * 256 * DM); else DW[di] = u32x4{0u, 0u, 0u, 0u}; } \
      } SB(); }
    KSTEP(0) KSTEP(1) KSTEP(2) KSTEP(3) KSTEP(4) KSTEP(5) KSTEP(6) KSTEP(7)
#undef KSTEP
#undef KDOT
#pragma unroll
    for (int k = 0; k < 4; ++k) {
      const float t1 = xs[k] + dpp<ROR8>(xs[k]), t2 = xs[k + 4] + dpp<ROR8>(xs[k + 4]);
      xs[k] = (c & 8) ? t2 : t1;
    }
#pragma unroll
    for (int k = 0; k < 2; ++k) {
      const float t1 = xs[k] + dpp<XOR7>(xs[k]), t2 = xs[k + 2] + dpp<XOR7>(xs[k + 2]);
      xs[k] = (c & 4) ? t2 : t1;
    }
    {
      const float t1 = xs[0] + dpp<XOR2>(xs[0]), t2 = xs[1] + dpp<XOR2>(xs[1]);
      float s = (c & 2) ? t2 : t1;
      s += dpp<XOR1>(s);
      sc[g] = rbf(rbf(s) * 0.08838834764831845f);
    }
  }

  STAMP(2);
  {
    float m = sc[0];
#pragma unroll
    for (int g = 1; g < 16; ++g) m = fmaxf(m, sc[g]);
    m = wave_max(m);
    float l = 0.f;
#pragma unroll
    for (int g = 0; g < 16; ++g) l += expf(sc[g] - m);
    l = wave_sum(l) * 0.5f;
    if (lane == 0) *reinterpret_cast<f32x2*>(lds + L_ST + w * 8) = f32x2{m, l};
  }
  __syncthreads();
  {
    const f32x2 st = *reinterpret_cast<const f32x2*>(lds + L_ST + (c & 7) * 8);
    const float M = row_max(st[0]);
    const float L = row_sum(st[1] * expf(st[0] - M)) * 0.5f;
    if ((c & 1) == 0) {
#pragma unroll
      for (int g = 0; g < 16; ++g) {
        const __bf16 p = (__bf16)(expf(sc[g] - M) / L);
        *reinterpret_cast<u16*>(lds + L_P + ((w * 16 + g) * 4 + r) * 16 + (c >> 1) * 2) = bc<u16>(p);
      }
    }
  }

  STAMP(3);
  float acc[8] = {0.f, 0.f, 0.f, 0.f, 0.f, 0.f, 0.f, 0.f};
#pragma unroll
  for (int g = 0; g < 16; ++g) {
    const u32x4 pw = *reinterpret_cast<const u32x4*>(lds + L_P + ((w * 16 + g) * 4 + r) * 16);
    const int sc0 = GOFF(g), sn = GOFF(g + 1);
#define VSTEP(ip) { mv2(R[2 * ip], R[2 * ip + 1], pw[ip], acc); SB(); \
      if (ip < 2) { R[(2 * ip + 4) & 7] = ldkv<(2 * ip + 4) & 7>(rV, voff, sc0); R[(2 * ip + 5) & 7] = ldkv<(2 * ip + 5) & 7>(rV, voff, sc0); } \
      else if (g < 15) { R[(2 * ip + 4) & 7] = ldkv<(2 * ip + 4) & 7>(rV, voff, sn); R[(2 * ip + 5) & 7] = ldkv<(2 * ip + 5) & 7>(rV, voff, sn); } SB(); }
    VSTEP(0) VSTEP(1) VSTEP(2) VSTEP(3)
#undef VSTEP
  }
  STAMP(4);
  const int ln2 = opaque(lane), r2 = ln2 >> 4, c2 = ln2 & 15, td2 = w * 64 + ln2;
  const u32x4 xr2 = *reinterpret_cast<const u32x4*>(x + b * DM + ln2 * 8);
  {
    float y[4];
#pragma unroll
    for (int k = 0; k < 4; ++k) y[k] = swapadd32(acc[k], acc[k + 4]);
    const float z0 = swapadd16(y[0], y[2]), z1 = swapadd16(y[1], y[3]);
    *reinterpret_cast<f32x2*>(lds + L_QP + (w * DH + 8 * c2 + 4 * (ln2 >> 5) + 2 * (r2 & 1)) * 4) = f32x2{z0, z1};
  }
  __syncthreads();
  if (td2 < DH) {
    const float* op = reinterpret_cast<const float*>(lds + L_QP);
    float s = 0.f;
#pragma unroll
    for (int k = 0; k < NW; ++k) s += op[k * DH + td2];
    const __bf16 o = (__bf16)s;
    *reinterpret_cast<u16*>(lds + L_OF + ((td2 & 7) * 16 + (td2 >> 3)) * 2) = bc<u16>(o);
  }
  __syncthreads();
  {
    const u32x4 ov0 = *reinterpret_cast<const u32x4*>(lds + L_OF + (ln2 >> 3) * 32);
    const u32x4 ov1 = *reinterpret_cast<const u32x4*>(lds + L_OF + (ln2 >> 3) * 32 + 16);
    float pa[8] = {0.f, 0.f, 0.f, 0.f, 0.f, 0.f, 0.f, 0.f};
#pragma unroll
    for (int e = 0; e < 4; ++e) mv2(A[2 * e], A[2 * e + 1], ov0[e], pa);
#pragma unroll
    for (int e = 0; e < 4; ++e) mv2(A[8 + 2 * e], A[8 + 2 * e + 1], ov1[e], pa);
    float y[4];
#pragma unroll
    for (int k = 0; k < 4; ++k) y[k] = swapadd32(pa[k], pa[k + 4]);
    float z0 = swapadd16(y[0], y[2]), z1 = swapadd16(y[1], y[3]);
    z0 += dpp<ROR8>(z0); z1 += dpp<ROR8>(z1);
    if ((ln2 & 8) == 0)
      st_sc1_b64(ws + WS_PART + ((size_t)(b * NH + h) * DM + 64 * w + 8 * (ln2 & 7) + 4 * (ln2 >> 5) + 2 * (r2 & 1)) * 4, z0, z1);
  }
  STAMP(5);

  {
    const __amdgpu_buffer_rsrc_t rs = __builtin_amdgcn_make_buffer_rsrc((void*)(ws + WS_PART + (size_t)b * NH * DM * 4), 0, NH * DM * 4, 0x00020000);
    const int off0 = (r2 * DM + 64 * w + 4 * c2) * 4;
    u32x4 v[8];
    bool done[8] = {false, false, false, false, false, false, false, false};
    for (unsigned spin = 0;; ++spin) {
      asm volatile("" ::: "memory");
#pragma unroll
      for (int i = 0; i < 8; ++i) if (!done[i]) v[i] = __builtin_amdgcn_raw_buffer_load_b128(rs, off0 + i * 4 * DM * 4, 0, 16);
      bool all = true;
#pragma unroll
      for (int i = 0; i < 8; ++i) { if (!done[i]) done[i] = __all(v[i][0] != SENT && v[i][1] != SENT && v[i][2] != SENT && v[i][3] != SENT); all = all && done[i]; }
      if (all || spin > SPIN_CAP) break;
      __builtin_amdgcn_s_sleep(1);
    }
    STAMP(6);
    float s[4];
#pragma unroll
    for (int k = 0; k < 4; ++k) {
      s[k] = bc<float>(v[0][k]);
#pragma unroll
      for (int i = 1; i < 8; ++i) s[k] += bc<float>(v[i][k]);
    }
    const float y0 = swapadd32(s[0], s[2]), y1 = swapadd32(s[1], s[3]);
    const float z = swapadd16(y0, y1);
    reinterpret_cast<float*>(lds + L_HS)[64 * w + 4 * c2 + 2 * (ln2 >> 5) + (r2 & 1)] = z;
  }
  __syncthreads();
  {
    float hv[8];
    const f32x4* hs = reinterpret_cast<const f32x4*>(lds + L_HS);
    const f32x4 a0 = hs[2 * ln2], a1 = hs[2 * ln2 + 1];
#pragma unroll
    for (int k = 0; k < 4; ++k) {
      const float xa = (k & 1) ? bfhi(xr2[k >> 1]) : bflo(xr2[k >> 1]);
      const float xb = (k & 1) ? bfhi(xr2[2 + (k >> 1)]) : bflo(xr2[2 + (k >> 1)]);
      hv[k] = rbf(xa + rbf(a0[k]));
      hv[4 + k] = rbf(xb + rbf(a1[k]));
    }
    if (w == 0 && (ln2 >> 1) == h) {
      float* hr = reinterpret_cast<float*>(lds + L_HR) + (ln2 & 1) * 8;
#pragma unroll
      for (int k = 0; k < 8; ++k) hr[k] = hv[k];
    }
    unsigned hp[4];
    ln_row(hv, hp);
    float ga[8] = {0.f, 0.f, 0.f, 0.f, 0.f, 0.f, 0.f, 0.f};
#pragma unroll
    for (int e = 0; e < 8; ++e) {
      const unsigned xp = (unsigned)__shfl((int)hp[e & 3], 8 * w + 2 * r2 + (e >> 2));
      mv2(X[2 * e], X[2 * e + 1], xp, ga);
    }
    float y[4];
#pragma unroll
    for (int k = 0; k < 4; ++k) y[k] = swapadd32(ga[k], ga[k + 4]);
    const float z0 = swapadd16(y[0], y[2]), z1 = swapadd16(y[1], y[3]);
    if (c2 < 2 * nch) *reinterpret_cast<f32x2*>(lds + L_GP + (w * 96 + c2 * 8 + 4 * (ln2 >> 5) + 2 * (r2 & 1)) * 4) = f32x2{z0, z1};
  }
  __syncthreads();
  if (w == 0 && ln2 < 8 * nch) {
    const float* gp = reinterpret_cast<const float*>(lds + L_GP);
    float g = 0.f, u = 0.f;
#pragma unroll
    for (int k = 0; k < NW; ++k) { g += gp[k * 96 + ln2]; u += gp[k * 96 + 8 * nch + ln2]; }
    g = rbf(g); u = rbf(u);
    const float sg = rbf(g / (1.f + expf(-g)));
    const float a = rbf(sg * u);
    unsigned* ap = reinterpret_cast<unsigned*>(ws + WS_ACT) + b * ACT_LD + chunk0(h) * 8 + ln2;
    __hip_atomic_store(ap, bc<unsigned>(a) & 0xffff0000u, __ATOMIC_RELAXED, __HIP_MEMORY_SCOPE_AGENT);
  }
  STAMP(7);

  {
    const bool has6 = w < 3;
    const unsigned* ap = reinterpret_cast<const unsigned*>(ws + WS_ACT) + b * ACT_LD + (td2 >> 1);
    unsigned a[6];
    for (unsigned spin = 0;; ++spin) {
      bool ok = true;
#pragma unroll
      for (int i = 0; i < 5; ++i) { a[i] = __hip_atomic_load(ap + 256 * i, __ATOMIC_RELAXED, __HIP_MEMORY_SCOPE_AGENT); }
      a[5] = has6 ? __hip_atomic_load(ap + 1280, __ATOMIC_RELAXED, __HIP_MEMORY_SCOPE_AGENT) : 0u;
#pragma unroll
      for (int i = 0; i < 6; ++i) ok = ok && a[i] != SENT;
      if (__all(ok) || spin > SPIN_CAP) break;
      __builtin_amdgcn_s_sleep(1);
    }
    STAMP(8);
    float da[8];
#pragma unroll
    for (int k = 0; k < 4; ++k) {
      da[2 * k] = bc<float>(a[0]) * bflo(DW[0][k]); da[2 * k + 1] = bc<float>(a[0]) * bfhi(DW[0][k]);
#pragma unroll
      for (int i = 1; i < 6; ++i) {
        da[2 * k] = fmaf(bc<float>(a[i]), bflo(DW[i][k]), da[2 * k]);
        da[2 * k + 1] = fmaf(bc<float>(a[i]), bfhi(DW[i][k]), da[2 * k + 1]);
      }
    }
    float y[4];
#pragma unroll
    for (int k = 0; k < 4; ++k) y[k] = swapadd32(da[k], da[k + 4]);
    float z0 = swapadd16(y[0], y[2]), z1 = swapadd16(y[1], y[3]);
    z0 += dpp<ROR8>(z0); z1 += dpp<ROR8>(z1);
    z0 += dpp<ROR4>(z0); z1 += dpp<ROR4>(z1);
    z0 += dpp<XOR2>(z0); z1 += dpp<XOR2>(z1);
    if ((ln2 & 14) == 0)
      *reinterpret_cast<f32x2*>(lds + L_DP + (w * 16 + 8 * (ln2 & 1) + 4 * (ln2 >> 5) + 2 * (r2 & 1)) * 4) = f32x2{z0, z1};
  }
  __syncthreads();
  if (td2 < 16) {
    const float* dp = reinterpret_cast<const float*>(lds + L_DP);
    float s = 0.f;
#pragma unroll
    for (int k = 0; k < NW; ++k) s += dp[k * 16 + td2];
    const float hres = reinterpret_cast<const float*>(lds + L_HR)[td2];
    const __bf16 o = (__bf16)(hres + rbf(s));
    out[b * DM + 16 * h + td2] = bc<u16>(o);
  }
  STAMP(9);
  TSFLUSH;
#undef GOFF
}

}

extern "C" void kernel_launch(void* const* d_in, const int* in_sizes, int n_in,
                              void* d_out, int out_size, void* d_ws, size_t ws_size,
                              hipStream_t stream) {
  const u16* x = (const u16*)d_in[0];
  const u16* kc = (const u16*)d_in[1];
  const u16* vc = (const u16*)d_in[2];
  const u16* wq = (const u16*)d_in[3];
  const u16* wo = (const u16*)d_in[4];
  const u16* wg = (const u16*)d_in[6];
  const u16* wu = (const u16*)d_in[7];
  const u16* wd = (const u16*)d_in[8];
  u16* out = (u16*)d_out;
  unsigned char* ws = (unsigned char*)d_ws;
  (void)hipMemsetAsync(ws, 0xFF, WS_INIT_BYTES, stream);
  hipLaunchKernelGGL(fused_kernel, dim3(NB * NH), dim3(NT), 0, stream, x, kc, vc, wq, wo, wg, wu, wd, out, ws);
}
```

```cpp
#include <hip/hip_runtime.h>
#include <hip/hip_bf16.h>
#include <stdint.h>

#define WL_GU 1
#define WL_WO 1
#define WL_WD 1
#define WLD(p) __builtin_nontemporal_load(reinterpret_cast<const u32x4*>(p))
#define QLD(p) __builtin_nontemporal_load(reinterpret_cast<const u32x4*>(p))
#define KV_NT 1

#define STAMP(k)
#define TSARG
#define TSPASS
#define TSDECL
#define TSFLUSH

namespace {

constexpr int NB = 8, DM = 512, NH = 32, SK = 4096, DH = 128, FF = 1376;
constexpr int NW = 8, NT = NW * 64;
constexpr unsigned SENT = 0xFFFFFFFFu;
constexpr unsigned SPIN_CAP = 400000u;

using u16 = unsigned short;
using u32x4 = __attribute__((ext_vector_type(4))) unsigned;
using u32x2 = __attribute__((ext_vector_type(2))) unsigned;
using f32x4 = __attribute__((ext_vector_type(4))) float;
using f32x2 = __attribute__((ext_vector_type(2))) float;
using bf16x2 = __attribute__((ext_vector_type(2))) __bf16;

constexpr size_t WS_PART = 0;
constexpr size_t PART_BYTES = (size_t)NB * NH * DM * 4;
constexpr int ACT_LD = 1408;
constexpr size_t WS_ACT = WS_PART + PART_BYTES;
constexpr size_t ACT_BYTES = (size_t)NB * ACT_LD * 4;
constexpr size_t WS_INIT_BYTES = PART_BYTES + ACT_BYTES;
constexpr size_t WS_H = WS_ACT + ACT_BYTES;

constexpr int L_P = 0;
constexpr int L_QP = L_P + 8192;
constexpr int L_QF = L_QP + 4096;
constexpr int L_ST = L_QF + 256;
constexpr int L_OF = L_ST + 128;
constexpr int L_HS = L_OF + 256;
constexpr int L_HR = L_HS + 2048;
constexpr int L_GP = L_HR + 64;
constexpr int L_DP = L_GP + 8 * 96 * 4;
constexpr int L_END = L_DP + 512;

template <typename T, typename F> __device__ __forceinline__ T bc(F x) { return __builtin_bit_cast(T, x); }
__device__ __forceinline__ float rbf(float x) { return (float)(__bf16)x; }
__device__ __forceinline__ unsigned pk2(float lo, float hi) { bf16x2 v = {(__bf16)lo, (__bf16)hi}; return bc<unsigned>(v); }
__device__ __forceinline__ float bflo(unsigned u) { return bc<float>(u << 16); }
__device__ __forceinline__ float bfhi(unsigned u) { return bc<float>(u & 0xffff0000u); }
__device__ __forceinline__ float dot2(unsigned a, unsigned b, float c) {
  return __builtin_amdgcn_fdot2_f32_bf16(bc<bf16x2>(a), bc<bf16x2>(b), c, false);
}
template <int CTRL> __device__ __forceinline__ float dpp(float x) {
  return bc<float>(__builtin_amdgcn_mov_dpp(bc<int>(x), CTRL, 0xf, 0xf, true));
}
constexpr int XOR1 = 0xB1, XOR2 = 0x4E, XOR7 = 0x141, ROR8 = 0x128, ROR4 = 0x124;
__device__ __forceinline__ float swapadd32(float a, float b) {
  auto s = __builtin_amdgcn_permlane32_swap(bc<unsigned>(a), bc<unsigned>(b), false, false);
  return bc<float>(s[0]) + bc<float>(s[1]);
}
__device__ __forceinline__ float swapadd16(float a, float b) {
  auto s = __builtin_amdgcn_permlane16_swap(bc<unsigned>(a), bc<unsigned>(b), false, false);
  return bc<float>(s[0]) + bc<float>(s[1]);
}
__device__ __forceinline__ float swapmax32(float a) {
  auto s = __builtin_amdgcn_permlane32_swap(bc<unsigned>(a), bc<unsigned>(a), false, false);
  return fmaxf(bc<float>(s[0]), bc<float>(s[1]));
}
__device__ __forceinline__ float swapmax16(float a) {
  auto s = __builtin_amdgcn_permlane16_swap(bc<unsigned>(a), bc<unsigned>(a), false, false);
  return fmaxf(bc<float>(s[0]), bc<float>(s[1]));
}
__device__ __forceinline__ float row_sum(float x) {
  x += dpp<XOR1>(x); x += dpp<XOR2>(x); x += dpp<XOR7>(x); x += dpp<ROR8>(x); return x;
}
__device__ __forceinline__ float row_max(float x) {
  x = fmaxf(x, dpp<XOR1>(x)); x = fmaxf(x, dpp<XOR2>(x)); x = fmaxf(x, dpp<XOR7>(x)); x = fmaxf(x, dpp<ROR8>(x)); return x;
}
__device__ __forceinline__ float wave_sum(float x) { x = row_sum(x); x = swapadd16(x, x); return swapadd32(x, x); }
__device__ __forceinline__ float wave_max(float x) { x = row_max(x); x = swapmax16(x); return swapmax32(x); }

__device__ __forceinline__ void mv2(const u32x4& w0, const u32x4& w1, unsigned xp, float (&acc)[8]) {
#pragma unroll
  for (int k = 0; k < 4; ++k) {
    const unsigned lo = __builtin_amdgcn_perm(w1[k], w0[k], 0x05040100u);
    const unsigned hi = __builtin_amdgcn_perm(w1[k], w0[k], 0x07060302u);
    acc[2 * k] = dot2(lo, xp, acc[2 * k]);
    acc[2 * k + 1] = dot2(hi, xp, acc[2 * k + 1]);
  }
}

__device__ __forceinline__ void ln_row(const float (&v)[8], unsigned (&o)[4]) {
  float s = 0.f;
#pragma unroll
  for (int k = 0; k < 8; ++k) s += v[k];
  const float mean = wave_sum(s) * (1.f / DM);
  float q = 0.f;
#pragma unroll
  for (int k = 0; k < 8; ++k) { const float d = v[k] - mean; q = fmaf(d, d, q); }
  const float var = wave_sum(q) * (1.f / DM);
  const float mb = rbf(mean);
  const float den = rbf(sqrtf(rbf(rbf(var) + 1e-5f)));
#pragma unroll
  for (int k = 0; k < 4; ++k) o[k] = pk2(rbf(v[2 * k] - mb) / den, rbf(v[2 * k + 1] - mb) / den);
}

template <int I>
__device__ __forceinline__ u32x4 ldkv(__amdgpu_buffer_rsrc_t rs, int voff, int soff) {
  return __builtin_amdgcn_raw_buffer_load_b128(rs, voff + (I & 3) * 1024, soff + (I >> 2) * 4096, KV_NT ? 2 : 0);
}
__device__ __forceinline__ int opaque(int v) { asm volatile("" : "+v"(v)); return v; }
#define SB() __builtin_amdgcn_sched_barrier(0)

__device__ __forceinline__ void st_sc1_b64(void* p, float a, float b) {
  unsigned ua = bc<unsigned>(a), ub = bc<unsigned>(b);
  ua = (ua == SENT) ? 0x7FC00000u : ua; ub = (ub == SENT) ? 0x7FC00000u : ub;
  __hip_atomic_store(reinterpret_cast<unsigned long long*>(p), ((unsigned long long)ub << 32) | ua, __ATOMIC_RELAXED, __HIP_MEMORY_SCOPE_AGENT);
}

__device__ __forceinline__ int chunk0(int j) { return 5 * j + (j < 12 ? j : 12); }

__global__ void __launch_bounds__(NT, 2)
fused_kernel(const u16* __restrict__ x, const u16* __restrict__ kc, const u16* __restrict__ vc, const u16* __restrict__ wq,
             const u16* __restrict__ wo, const u16* __restrict__ wg, const u16* __restrict__ wu, const u16* __restrict__ wd,
             u16* __restrict__ out, unsigned char* __restrict__ ws) {
  __shared__ __attribute__((aligned(16))) unsigned char lds[L_END];
  const int tid = threadIdx.x, lane = tid & 63;
  const int w = __builtin_amdgcn_readfirstlane(tid >> 6);
  const int r = lane >> 4, c = lane & 15;
  const int bid = blockIdx.x;
  const int h = (bid & 7) * 4 + ((bid >> 3) & 3), b = bid >> 5;
#define RA 16
#define RB 0
#define RC 2
  const int rot8 = RA * (bid & 7) + RB * ((bid >> 3) & 3) + RC * (bid >> 5) + w;
  const int nch = h < 12 ? 6 : 5;

  const __amdgpu_buffer_rsrc_t rK = __builtin_amdgcn_make_buffer_rsrc((void*)(kc + (size_t)(b * NH + h) * SK * DH), 0, SK * DH * 2, 0x00020000);
  const __amdgpu_buffer_rsrc_t rV = __builtin_amdgcn_make_buffer_rsrc((void*)(vc + (size_t)(b * NH + h) * SK * DH), 0, SK * DH * 2, 0x00020000);
  const int voff = lane * 16;
#define GOFF(g) (((((g) & 15) * 8 + rot8) & 127) * 8192)

  TSDECL
  STAMP(0);
  const u32x4 xr = *reinterpret_cast<const u32x4*>(x + b * DM + lane * 8);
  u32x4 A[16], X[16], R[8], DW[6];
  {
    const u16* base = wq + (size_t)(64 * w + 16 * r) * (NH * DH) + h * DH + 8 * c;
#pragma unroll
    for (int e = 0; e < 16; ++e) A[e] = QLD(base + e * (NH * DH));
  }
  SB();
#define LD8(dst, o, rs, so) dst[o + 0] = ldkv<0>(rs, voff, so); dst[o + 1] = ldkv<1>(rs, voff, so); dst[o + 2] = ldkv<2>(rs, voff, so); dst[o + 3] = ldkv<3>(rs, voff, so); \
                            dst[o + 4] = ldkv<4>(rs, voff, so); dst[o + 5] = ldkv<5>(rs, voff, so); dst[o + 6] = ldkv<6>(rs, voff, so); dst[o + 7] = ldkv<7>(rs, voff, so);
  LD8(R, 0, rK, GOFF(0)) LD8(X, 0, rK, GOFF(1)) LD8(X, 8, rK, GOFF(2))
#undef LD8
  SB();

  {
    float xv[8];
#pragma unroll
    for (int k = 0; k < 4; ++k) { xv[2 * k] = bflo(xr[k]); xv[2 * k + 1] = bfhi(xr[k]); }
    unsigned xn[4];
    ln_row(xv, xn);
    float acc[8] = {0.f, 0.f, 0.f, 0.f, 0.f, 0.f, 0.f, 0.f};
#pragma unroll
    for (int e = 0; e < 8; ++e) {
      const unsigned xp = (unsigned)__shfl((int)xn[e & 3], 8 * w + 2 * r + (e >> 2));
      mv2(A[2 * e], A[2 * e + 1], xp, acc);
    }
    float y[4];
#pragma unroll
    for (int k = 0; k < 4; ++k) y[k] = swapadd32(acc[k], acc[k + 4]);
    const float z0 = swapadd16(y[0], y[2]), z1 = swapadd16(y[1], y[3]);
    *reinterpret_cast<f32x2*>(lds + L_QP + (w * DH + 8 * c + 4 * (lane >> 5) + 2 * (r & 1)) * 4) = f32x2{z0, z1};
  }
  __syncthreads();
  if (tid < 64) {
    const f32x2* qp = reinterpret_cast<const f32x2*>(lds + L_QP);
    float a0 = 0.f, a1 = 0.f;
#pragma unroll
    for (int k = 0; k < NW; ++k) { const f32x2 t = qp[k * 64 + tid]; a0 += t[0]; a1 += t[1]; }
    reinterpret_cast<unsigned*>(lds + L_QF)[tid] = pk2(a0, a1);
  }
  __syncthreads();
  const u32x4 q = *reinterpret_cast<const u32x4*>(lds + L_QF + c * 16);

  STAMP(1);
  const u16* gub;
  {
    int cm = c < 2 * nch ? c : 2 * nch - 1;
    const bool up = cm >= nch;
    gub = (up ? wu : wg) + (size_t)(64 * w + 16 * r) * FF + (chunk0(h) + (up ? cm - nch : cm)) * 8;
  }
  float sc[16];
#pragma unroll
  for (int g = 0; g < 16; ++g) {
    float xs[8];
    const int sc0 = GOFF(g);
    const __amdgpu_buffer_rsrc_t rn = (g < 15) ? rK : rV;
    const int sn = (g < 15) ? GOFF(g + 1) : GOFF(0);
    const int l1 = opaque(lane);
    const u16* wob = wo + (size_t)(h * DH + (l1 >> 3)) * DM + 64 * w + 8 * (l1 & 7);
    const u16* wdb = wd + (size_t)((w * 64 + l1) >> 1) * DM + 16 * h + 8 * (l1 & 1);
#define KDOT(src) { float d = dot2(src[0], q[0], 0.f); d = dot2(src[1], q[1], d); d = dot2(src[2], q[2], d); d = dot2(src[3], q[3], d); xs[i_] = d; }
#define KSTEP(i) { constexpr int i_ = i; \
      if (g == 1) { KDOT(X[i]) SB(); if (WL_GU) X[i] = WLD(gub + (i) * FF); } \
      else if (g == 2) { KDOT(X[8 + i]) SB(); if (WL_GU) X[8 + i] = WLD(gub + (8 + i) * FF); } \
      else { KDOT(R[i]) SB(); \
        if (g == 0) R[i] = ldkv<i>(rK, voff, GOFF(3)); \
        else if (i < 4) { if (g > 3) R[(i + 4) & 7] = ldkv<(i + 4) & 7>(rK, voff, sc0); } \
        else R[(i + 4) & 7] = ldkv<(i + 4) & 7>(rn, voff, sn); \
        if (WL_WO && g >= 4 && g < 12 && (i & 3) == 1) A[2 * (g - 4) + (i >> 2)] = WLD(wob + (2 * (g - 4) + (i >> 2)) * 8 * DM); \
        if (WL_WD && g >= 12 && g < 15 && (i & 3) == 1) { constexpr int dd = (i >> 2); const int di = 2 * (g - 12) + dd; \
          if (di < 5 || w < 3) DW[di] = WLD(wdb + di * 256 * DM); else DW[di] = u32x4{0u, 0u, 0u, 0u}; } \
      } SB(); }
    KSTEP(0) KSTEP(1) KSTEP(2) KSTEP(3) KSTEP(4) KSTEP(5) KSTEP(6) KSTEP(7)
#undef KSTEP
#undef KDOT
#pragma unroll
    for (int k = 0; k < 4; ++k) {
      const float t1 = xs[k] + dpp<ROR8>(xs[k]), t2 = xs[k + 4] + dpp<ROR8>(xs[k + 4]);
      xs[k] = (c & 8) ? t2 : t1;
    }
#pragma unroll
    for (int k = 0; k < 2; ++k) {
      const float t1 = xs[k] + dpp<XOR7>(xs[k]), t2 = xs[k + 2] + dpp<XOR7>(xs[k + 2]);
      xs[k] = (c & 4) ? t2 : t1;
    }
    {
      const float t1 = xs[0] + dpp<XOR2>(xs[0]), t2 = xs[1] + dpp<XOR2>(xs[1]);
      float s = (c & 2) ? t2 : t1;
      s += dpp<XOR1>(s);
      sc[g] = rbf(rbf(s) * 0.08838834764831845f);
    }
  }

  STAMP(2);
  {
    float m = sc[0];
#pragma unroll
    for (int g = 1; g < 16; ++g) m = fmaxf(m, sc[g]);
    m = wave_max(m);
    float l = 0.f;
#pragma unroll
    for (int g = 0; g < 16; ++g) l += expf(sc[g] - m);
    l = wave_sum(l) * 0.5f;
    if (lane == 0) *reinterpret_cast<f32x2*>(lds + L_ST + w * 8) = f32x2{m, l};
  }
  __syncthreads();
  {
    const f32x2 st = *reinterpret_cast<const f32x2*>(lds + L_ST + (c & 7) * 8);
    const float M = row_max(st[0]);
    const float L = row_sum(st[1] * expf(st[0] - M)) * 0.5f;
    if ((c & 1) == 0) {
#pragma unroll
      for (int g = 0; g < 16; ++g) {
        const __bf16 p = (__bf16)(expf(sc[g] - M) / L);
        *reinterpret_cast<u16*>(lds + L_P + ((w * 16 + g) * 4 + r) * 16 + (c >> 1) * 2) = bc<u16>(p);
      }
    }
  }

  STAMP(3);
  float acc[8] = {0.f, 0.f, 0.f, 0.f, 0.f, 0.f, 0.f, 0.f};
#pragma unroll
  for (int g = 0; g < 16; ++g) {
    const u32x4 pw = *reinterpret_cast<const u32x4*>(lds + L_P + ((w * 16 + g) * 4 + r) * 16);
    const int sc0 = GOFF(g), sn = GOFF(g + 1);
#define VSTEP(ip) { mv2(R[2 * ip], R[2 * ip + 1], pw[ip], acc); SB(); \
      if (ip < 2) { R[(2 * ip + 4) & 7] = ldkv<(2 * ip + 4) & 7>(rV, voff, sc0); R[(2 * ip + 5) & 7] = ldkv<(2 * ip + 5) & 7>(rV, voff, sc0); } \
      else if (g < 15) { R[(2 * ip + 4) & 7] = ldkv<(2 * ip + 4) & 7>(rV, voff, sn); R[(2 * ip + 5) & 7] = ldkv<(2 * ip + 5) & 7>(rV, voff, sn); } SB(); }
    VSTEP(0) VSTEP(1) VSTEP(2) VSTEP(3)
#undef VSTEP
  }
  STAMP(4);
  const int ln2 = opaque(lane), r2 = ln2 >> 4, c2 = ln2 & 15, td2 = w * 64 + ln2;
  const u32x4 xr2 = *reinterpret_cast<const u32x4*>(x + b * DM + ln2 * 8);
  {
    const u16* wob = wo + (size_t)(h * DH + (ln2 >> 3)) * DM + 64 * w + 8 * (ln2 & 7);
    const u16* wdb = wd + (size_t)((w * 64 + ln2) >> 1) * DM + 16 * h + 8 * (ln2 & 1);
    if (!WL_WO) {
#pragma unroll
      for (int e = 0; e < 16; ++e) A[e] = WLD(wob + e * 8 * DM);
    }
    if (!WL_GU) {
#pragma unroll
      for (int e = 0; e < 16; ++e) X[e] = WLD(gub + e * FF);
    }
    if (!WL_WD) {
#pragma unroll
      for (int di = 0; di < 6; ++di) { DW[di] = WLD(wdb + (di < 5 || w < 3 ? di : 0) * 256 * DM); if (di == 5 && w >= 3) DW[di] = u32x4{0u, 0u, 0u, 0u}; }
    }
  }
  {
    float y[4];
#pragma unroll
    for (int k = 0; k < 4; ++k) y[k] = swapadd32(acc[k], acc[k + 4]);
    const float z0 = swapadd16(y[0], y[2]), z1 = swapadd16(y[1], y[3]);
    *reinterpret_cast<f32x2*>(lds + L_QP + (w * DH + 8 * c2 + 4 * (ln2 >> 5) + 2 * (r2 & 1)) * 4) = f32x2{z0, z1};
  }
  __syncthreads();
  if (td2 < DH) {
    const float* op = reinterpret_cast<const float*>(lds + L_QP);
    float s = 0.f;
#pragma unroll
    for (int k = 0; k < NW; ++k) s += op[k * DH + td2];
    const __bf16 o = (__bf16)s;
    *reinterpret_cast<u16*>(lds + L_OF + ((td2 & 7) * 16 + (td2 >> 3)) * 2) = bc<u16>(o);
  }
  __syncthreads();
  {
    const u32x4 ov0 = *reinterpret_cast<const u32x4*>(lds + L_OF + (ln2 >> 3) * 32);
    const u32x4 ov1 = *reinterpret_cast<const u32x4*>(lds + L_OF + (ln2 >> 3) * 32 + 16);
    float pa[8] = {0.f, 0.f, 0.f, 0.f, 0.f, 0.f, 0.f, 0.f};
#pragma unroll
    for (int e = 0; e < 4; ++e) mv2(A[2 * e], A[2 * e + 1], ov0[e], pa);
#pragma unroll
    for (int e = 0; e < 4; ++e) mv2(A[8 + 2 * e], A[8 + 2 * e + 1], ov1[e], pa);
    float y[4];
#pragma unroll
    for (int k = 0; k < 4; ++k) y[k] = swapadd32(pa[k], pa[k + 4]);
    float z0 = swapadd16(y[0], y[2]), z1 = swapadd16(y[1], y[3]);
    z0 += dpp<ROR8>(z0); z1 += dpp<ROR8>(z1);
    if ((ln2 & 8) == 0)
      st_sc1_b64(ws + WS_PART + ((size_t)(b * NH + h) * DM + 64 * w + 8 * (ln2 & 7) + 4 * (ln2 >> 5) + 2 * (r2 & 1)) * 4, z0, z1);
  }
  STAMP(5);

  {
    const __amdgpu_buffer_rsrc_t rs = __builtin_amdgcn_make_buffer_rsrc((void*)(ws + WS_PART + (size_t)b * NH * DM * 4), 0, NH * DM * 4, 0x00020000);
    const int off0 = (r2 * DM + 64 * w + 4 * c2) * 4;
    u32x4 v[8];
    bool done[8] = {false, false, false, false, false, false, false, false};
    for (unsigned spin = 0;; ++spin) {
      asm volatile("" ::: "memory");
#pragma unroll
      for (int i = 0; i < 8; ++i) if (!done[i]) v[i] = __builtin_amdgcn_raw_buffer_load_b128(rs, off0 + i * 4 * DM * 4, 0, 16);
      bool all = true;
#pragma unroll
      for (int i = 0; i < 8; ++i) { if (!done[i]) done[i] = __all(v[i][0] != SENT && v[i][1] != SENT && v[i][2] != SENT && v[i][3] != SENT); all = all && done[i]; }
      if (all || spin > SPIN_CAP) break;
      __builtin_amdgcn_s_sleep(1);
    }
    STAMP(6);
    float s[4];
#pragma unroll
    for (int k = 0; k < 4; ++k) {
      s[k] = bc<float>(v[0][k]);
#pragma unroll
      for (int i = 1; i < 8; ++i) s[k] += bc<float>(v[i][k]);
    }
    const float y0 = swapadd32(s[0], s[2]), y1 = swapadd32(s[1], s[3]);
    const float z = swapadd16(y0, y1);
    reinterpret_cast<float*>(lds + L_HS)[64 * w + 4 * c2 + 2 * (ln2 >> 5) + (r2 & 1)] = z;
  }
  __syncthreads();
  {
    float hv[8];
    const f32x4* hs = reinterpret_cast<const f32x4*>(lds + L_HS);
    const f32x4 a0 = hs[2 * ln2], a1 = hs[2 * ln2 + 1];
#pragma unroll
    for (int k = 0; k < 4; ++k) {
      const float xa = (k & 1) ? bfhi(xr2[k >> 1]) : bflo(xr2[k >> 1]);
      const float xb = (k & 1) ? bfhi(xr2[2 + (k >> 1)]) : bflo(xr2[2 + (k >> 1)]);
      hv[k] = rbf(xa + rbf(a0[k]));
      hv[4 + k] = rbf(xb + rbf(a1[k]));
    }
    if (w == 0 && (ln2 >> 1) == h) {
      float* hr = reinterpret_cast<float*>(lds + L_HR) + (ln2 & 1) * 8;
#pragma unroll
      for (int k = 0; k < 8; ++k) hr[k] = hv[k];
    }
    unsigned hp[4];
    ln_row(hv, hp);
    float ga[8] = {0.f, 0.f, 0.f, 0.f, 0.f, 0.f, 0.f, 0.f};
#pragma unroll
    for (int e = 0; e < 8; ++e) {
      const unsigned xp = (unsigned)__shfl((int)hp[e & 3], 8 * w + 2 * r2 + (e >> 2));
      mv2(X[2 * e], X[2 * e + 1], xp, ga);
    }
    float y[4];
#pragma unroll
    for (int k = 0; k < 4; ++k) y[k] = swapadd32(ga[k], ga[k + 4]);
    const float z0 = swapadd16(y[0], y[2]), z1 = swapadd16(y[1], y[3]);
    if (c2 < 2 * nch) *reinterpret_cast<f32x2*>(lds + L_GP + (w * 96 + c2 * 8 + 4 * (ln2 >> 5) + 2 * (r2 & 1)) * 4) = f32x2{z0, z1};
  }
  __syncthreads();
  if (w == 0 && ln2 < 8 * nch) {
    const float* gp = reinterpret_cast<const float*>(lds + L_GP);
    float g = 0.f, u = 0.f;
#pragma unroll
    for (int k = 0; k < NW; ++k) { g += gp[k * 96 + ln2]; u += gp[k * 96 + 8 * nch + ln2]; }
    g = rbf(g); u = rbf(u);
    const float sg = rbf(g / (1.f + expf(-g)));
    const float a = rbf(sg * u);
    unsigned* ap = reinterpret_cast<unsigned*>(ws + WS_ACT) + b * ACT_LD + chunk0(h) * 8 + ln2;
    __hip_atomic_store(ap, bc<unsigned>(a) & 0xffff0000u, __ATOMIC_RELAXED, __HIP_MEMORY_SCOPE_AGENT);
  }
  STAMP(7);

  {
    const bool has6 = w < 3;
    const unsigned* ap = reinterpret_cast<const unsigned*>(ws + WS_ACT) + b * ACT_LD + (td2 >> 1);
    unsigned a[6];
    for (unsigned spin = 0;; ++spin) {
      bool ok = true;
#pragma unroll
      for (int i = 0; i < 5; ++i) { a[i] = __hip_atomic_load(ap + 256 * i, __ATOMIC_RELAXED, __HIP_MEMORY_SCOPE_AGENT); }
      a[5] = has6 ? __hip_atomic_load(ap + 1280, __ATOMIC_RELAXED, __HIP_MEMORY_SCOPE_AGENT) : 0u;
#pragma unroll
      for (int i = 0; i < 6; ++i) ok = ok && a[i] != SENT;
      if (__all(ok) || spin > SPIN_CAP) break;
      __builtin_amdgcn_s_sleep(1);
    }
    STAMP(8);
    float da[8];
#pragma unroll
    for (int k = 0; k < 4; ++k) {
      da[2 * k] = bc<float>(a[0]) * bflo(DW[0][k]); da[2 * k + 1] = bc<float>(a[0]) * bfhi(DW[0][k]);
#pragma unroll
      for (int i = 1; i < 6; ++i) {
        da[2 * k] = fmaf(bc<float>(a[i]), bflo(DW[i][k]), da[2 * k]);
        da[2 * k + 1] = fmaf(bc<float>(a[i]), bfhi(DW[i][k]), da[2 * k + 1]);
      }
    }
    float y[4];
#pragma unroll
    for (int k = 0; k < 4; ++k) y[k] = swapadd32(da[k], da[k + 4]);
    float z0 = swapadd16(y[0], y[2]), z1 = swapadd16(y[1], y[3]);
    z0 += dpp<ROR8>(z0); z1 += dpp<ROR8>(z1);
    z0 += dpp<ROR4>(z0); z1 += dpp<ROR4>(z1);
    z0 += dpp<XOR2>(z0); z1 += dpp<XOR2>(z1);
    if ((ln2 & 14) == 0)
      *reinterpret_cast<f32x2*>(lds + L_DP + (w * 16 + 8 * (ln2 & 1) + 4 * (ln2 >> 5) + 2 * (r2 & 1)) * 4) = f32x2{z0, z1};
  }
  __syncthreads();
  if (td2 < 16) {
    const float* dp = reinterpret_cast<const float*>(lds + L_DP);
    float s = 0.f;
#pragma unroll
    for (int k = 0; k < NW; ++k) s += dp[k * 16 + td2];
    const float hres = reinterpret_cast<const float*>(lds + L_HR)[td2];
    const __bf16 o = (__bf16)(hres + rbf(s));
    out[b * DM + 16 * h + td2] = bc<u16>(o);
  }
  STAMP(9);
  TSFLUSH;
#undef GOFF
}

}

extern "C" void kernel_launch(void* const* d_in, const int* in_sizes, int n_in,
                              void* d_out, int out_size, void* d_ws, size_t ws_size,
                              hipStream_t stream) {
  const u16* x = (const u16*)d_in[0];
  const u16* kc = (const u16*)d_in[1];
  const u16* vc = (const u16*)d_in[2];
  const u16* wq = (const u16*)d_in[3];
  const u16* wo = (const u16*)d_in[4];
  const u16* wg = (const u16*)d_in[6];
  const u16* wu = (const u16*)d_in[7];
  const u16* wd = (const u16*)d_in[8];
  u16* out = (u16*)d_out;
  unsigned char* ws = (unsigned char*)d_ws;
  (void)hipMemsetAsync(ws, 0xFF, WS_INIT_BYTES, stream);
  hipLaunchKernelGGL(fused_kernel, dim3(NB * NH), dim3(NT), 0, stream, x, kc, vc, wq, wo, wg, wu, wd, out, ws);
}
```

```cpp
#include <hip/hip_runtime.h>
#include <hip/hip_bf16.h>
#include <stdint.h>

#define WO_G0 3
#define WO_PER 4
#define WLD(p) __builtin_nontemporal_load(reinterpret_cast<const u32x4*>(p))
#define KV_NT 1

#define STAMP(k)
#define TSARG
#define TSPASS
#define TSDECL
#define TSFLUSH

namespace {

constexpr int NB = 8, DM = 512, NH = 32, SK = 4096, DH = 128, FF = 1376;
constexpr int NW = 8, NT = NW * 64;
constexpr unsigned SENT = 0xFFFFFFFFu;
constexpr unsigned SPIN_CAP = 400000u;

using u16 = unsigned short;
using u32x4 = __attribute__((ext_vector_type(4))) unsigned;
using u32x2 = __attribute__((ext_vector_type(2))) unsigned;
using f32x4 = __attribute__((ext_vector_type(4))) float;
using f32x2 = __attribute__((ext_vector_type(2))) float;
using bf16x2 = __attribute__((ext_vector_type(2))) __bf16;

constexpr size_t WS_SLOT = 0;
constexpr size_t SLOT_BYTES = (size_t)NB * NH * 4096;
constexpr size_t WS_HS = WS_SLOT + SLOT_BYTES;

constexpr int L_P = 0;
constexpr int L_QP = L_P + 8192;
constexpr int L_QF = L_QP + 4096;
constexpr int L_ST = L_QF + 256;
constexpr int L_OF = L_ST + 128;
constexpr int L_HS = L_OF + 256;
constexpr int L_HR = L_HS + 2048;
constexpr int L_GP = L_HR + 64;
constexpr int L_END = L_GP + 8 * 96 * 4;

template <typename T, typename F> __device__ __forceinline__ T bc(F x) { return __builtin_bit_cast(T, x); }
__device__ __forceinline__ float rbf(float x) { return (float)(__bf16)x; }
__device__ __forceinline__ unsigned pk2(float lo, float hi) { bf16x2 v = {(__bf16)lo, (__bf16)hi}; return bc<unsigned>(v); }
__device__ __forceinline__ float bflo(unsigned u) { return bc<float>(u << 16); }
__device__ __forceinline__ float bfhi(unsigned u) { return bc<float>(u & 0xffff0000u); }
__device__ __forceinline__ float dot2(unsigned a, unsigned b, float c) {
  return __builtin_amdgcn_fdot2_f32_bf16(bc<bf16x2>(a), bc<bf16x2>(b), c, false);
}
template <int CTRL> __device__ __forceinline__ float dpp(float x) {
  return bc<float>(__builtin_amdgcn_mov_dpp(bc<int>(x), CTRL, 0xf, 0xf, true));
}
constexpr int XOR1 = 0xB1, XOR2 = 0x4E, XOR7 = 0x141, ROR8 = 0x128, ROR4 = 0x124;
__device__ __forceinline__ float swapadd32(float a, float b) {
  auto s = __builtin_amdgcn_permlane32_swap(bc<unsigned>(a), bc<unsigned>(b), false, false);
  return bc<float>(s[0]) + bc<float>(s[1]);
}
__device__ __forceinline__ float swapadd16(float a, float b) {
  auto s = __builtin_amdgcn_permlane16_swap(bc<unsigned>(a), bc<unsigned>(b), false, false);
  return bc<float>(s[0]) + bc<float>(s[1]);
}
__device__ __forceinline__ float swapmax32(float a) {
  auto s = __builtin_amdgcn_permlane32_swap(bc<unsigned>(a), bc<unsigned>(a), false, false);
  return fmaxf(bc<float>(s[0]), bc<float>(s[1]));
}
__device__ __forceinline__ float swapmax16(float a) {
  auto s = __builtin_amdgcn_permlane16_swap(bc<unsigned>(a), bc<unsigned>(a), false, false);
  return fmaxf(bc<float>(s[0]), bc<float>(s[1]));
}
__device__ __forceinline__ float row_sum(float x) {
  x += dpp<XOR1>(x); x += dpp<XOR2>(x); x += dpp<XOR7>(x); x += dpp<ROR8>(x); return x;
}
__device__ __forceinline__ float row_max(float x) {
  x = fmaxf(x, dpp<XOR1>(x)); x = fmaxf(x, dpp<XOR2>(x)); x = fmaxf(x, dpp<XOR7>(x)); x = fmaxf(x, dpp<ROR8>(x)); return x;
}
__device__ __forceinline__ float wave_sum(float x) { x = row_sum(x); x = swapadd16(x, x); return swapadd32(x, x); }
__device__ __forceinline__ float wave_max(float x) { x = row_max(x); x = swapmax16(x); return swapmax32(x); }

__device__ __forceinline__ void mv2(const u32x4& w0, const u32x4& w1, unsigned xp, float (&acc)[8]) {
#pragma unroll
  for (int k = 0; k < 4; ++k) {
    const unsigned lo = __builtin_amdgcn_perm(w1[k], w0[k], 0x05040100u);
    const unsigned hi = __builtin_amdgcn_perm(w1[k], w0[k], 0x07060302u);
    acc[2 * k] = dot2(lo, xp, acc[2 * k]);
    acc[2 * k + 1] = dot2(hi, xp, acc[2 * k + 1]);
  }
}

__device__ __forceinline__ void ln_row(const float (&v)[8], unsigned (&o)[4]) {
  float s = 0.f;
#pragma unroll
  for (int k = 0; k < 8; ++k) s += v[k];
  const float mean = wave_sum(s) * (1.f / DM);
  float q = 0.f;
#pragma unroll
  for (int k = 0; k < 8; ++k) { const float d = v[k] - mean; q = fmaf(d, d, q); }
  const float var = wave_sum(q) * (1.f / DM);
  const float mb = rbf(mean);
  const float den = rbf(sqrtf(rbf(rbf(var) + 1e-5f)));
#pragma unroll
  for (int k = 0; k < 4; ++k) o[k] = pk2(rbf(v[2 * k] - mb) / den, rbf(v[2 * k + 1] - mb) / den);
}

template <int I>
__device__ __forceinline__ u32x4 ldkv(__amdgpu_buffer_rsrc_t rs, int voff, int soff) {
  return __builtin_amdgcn_raw_buffer_load_b128(rs, voff + (I & 3) * 1024, soff + (I >> 2) * 4096, KV_NT ? 2 : 0);
}
__device__ __forceinline__ int opaque(int v) { asm volatile("" : "+v"(v)); return v; }
#define SB() __builtin_amdgcn_sched_barrier(0)

__device__ __forceinline__ void st_sc1_b64(void* p, float a, float b) {
  unsigned ua = bc<unsigned>(a), ub = bc<unsigned>(b);
  ua = (ua == SENT) ? 0x7FC00000u : ua; ub = (ub == SENT) ? 0x7FC00000u : ub;
  __hip_atomic_store(reinterpret_cast<unsigned long long*>(p), ((unsigned long long)ub << 32) | ua, __ATOMIC_RELAXED, __HIP_MEMORY_SCOPE_AGENT);
}

__device__ __forceinline__ int chunk0(int j) { return 5 * j + (j < 12 ? j : 12); }

__device__ __forceinline__ void pin(u32x4& v) { asm volatile("" : "+v"(v)); }
__device__ __forceinline__ unsigned ld32(const void* p) { return __hip_atomic_load(reinterpret_cast<const unsigned*>(p), __ATOMIC_RELAXED, __HIP_MEMORY_SCOPE_AGENT); }
__device__ __forceinline__ unsigned long long ld64(const void* p) { return __hip_atomic_load(reinterpret_cast<const unsigned long long*>(p), __ATOMIC_RELAXED, __HIP_MEMORY_SCOPE_AGENT); }
__device__ __forceinline__ void st32(void* p, unsigned v) { __hip_atomic_store(reinterpret_cast<unsigned*>(p), v, __ATOMIC_RELAXED, __HIP_MEMORY_SCOPE_AGENT); }
__device__ __forceinline__ void st64(void* p, unsigned long long v) { __hip_atomic_store(reinterpret_cast<unsigned long long*>(p), v, __ATOMIC_RELAXED, __HIP_MEMORY_SCOPE_AGENT); }
__device__ __forceinline__ unsigned rfl(unsigned v) { return (unsigned)__builtin_amdgcn_readfirstlane((int)v); }

__global__ void __launch_bounds__(NT, 2)
fused_kernel(const u16* __restrict__ x, const u16* __restrict__ kc, const u16* __restrict__ vc, const u16* __restrict__ wq,
             const u16* __restrict__ wo, const u16* __restrict__ wg, const u16* __restrict__ wu, const u16* __restrict__ wd,
             u16* __restrict__ out, unsigned char* __restrict__ ws) {
  __shared__ __attribute__((aligned(16))) unsigned char lds[L_END];
  const int tid = threadIdx.x, lane = tid & 63;
  const int w = __builtin_amdgcn_readfirstlane(tid >> 6);
  const int r = lane >> 4, c = lane & 15;
  const int bid = blockIdx.x;
  const int h = (bid & 7) * 4 + ((bid >> 3) & 3), b = bid >> 5;
#define RA 16
#define RB 0
#define RC 2
  const int rot8 = RA * (bid & 7) + RB * ((bid >> 3) & 3) + RC * (bid >> 5) + w;
  const int nch = h < 12 ? 6 : 5;

  const __amdgpu_buffer_rsrc_t rK = __builtin_amdgcn_make_buffer_rsrc((void*)(kc + (size_t)(b * NH + h) * SK * DH), 0, SK * DH * 2, 0x00020000);
  const __amdgpu_buffer_rsrc_t rV = __builtin_amdgcn_make_buffer_rsrc((void*)(vc + (size_t)(b * NH + h) * SK * DH), 0, SK * DH * 2, 0x00020000);
  const int voff = lane * 16;
#define GOFF(g) (((((g) & 15) * 8 + rot8) & 127) * 8192)

  TSDECL
  STAMP(0);
  unsigned char* const hsb = ws + WS_HS + (size_t)b * 1024;
  const bool lead = h == 0;
  st64(ws + WS_SLOT + (size_t)(b * NH + h) * 4096 + tid * 8, ~0ull);
  unsigned rv = ld32(hsb + 512 + h * 4), gv = 0u;
  unsigned long long ab = ld64(hsb + 256 + (lane & 31) * 8);
  SB();
  const u32x4 xr = *reinterpret_cast<const u32x4*>(x + b * DM + lane * 8);
  u32x4 A[16], X[16], R[8], DW[6];
  {
    const u16* base = wq + (size_t)(64 * w + 16 * r) * (NH * DH) + h * DH + 8 * c;
#pragma unroll
    for (int e = 0; e < 16; ++e) A[e] = WLD(base + e * (NH * DH));
  }
  SB();
#define LD8(dst, o, rs, so) dst[o + 0] = ldkv<0>(rs, voff, so); dst[o + 1] = ldkv<1>(rs, voff, so); dst[o + 2] = ldkv<2>(rs, voff, so); dst[o + 3] = ldkv<3>(rs, voff, so); \
                            dst[o + 4] = ldkv<4>(rs, voff, so); dst[o + 5] = ldkv<5>(rs, voff, so); dst[o + 6] = ldkv<6>(rs, voff, so); dst[o + 7] = ldkv<7>(rs, voff, so);
  LD8(R, 0, rK, GOFF(0)) LD8(X, 0, rK, GOFF(1)) LD8(X, 8, rK, GOFF(2))
#undef LD8
  SB();
  {
    const u16* wdb = wd + (size_t)(chunk0(h) * 8 + (lane >> 3)) * DM + 64 * w + 8 * (lane & 7);
#pragma unroll
    for (int i = 0; i < 6; ++i) DW[i] = WLD(wdb + (i < 5 || nch == 6 ? i : 0) * 8 * DM);
  }
  SB();
  const unsigned r1 = rfl(rv) + 1u;
  unsigned tagN = 0u, eg = 0u;
  bool ech = false, granted = false, ready = false;
  if (lead && w == 0) {
    while (__any((unsigned)ab == tagN)) ++tagN;
    if (lane == 0) st32(hsb, tagN);
  }
#define HS_INIT if (w == 0) { gv = ld32(hsb); rv = ld32(hsb + 512 + h * 4); }
#define HS_STEP if (w == 0 && !ready) { \
    const unsigned g_ = rfl(gv), r_ = rfl(rv); \
    if (r_ == r1) ready = true; \
    else { \
      if (!ech || g_ != eg) { if (lane == 0) st64(hsb + 256 + h * 8, (unsigned long long)g_ | ((unsigned long long)r1 << 32)); eg = g_; ech = true; } \
      if (lead && !granted) { if (__all((unsigned)ab == tagN)) { if (lane < 32) st32(hsb + 512 + lane * 4, (unsigned)(ab >> 32)); granted = true; } } \
      gv = ld32(hsb); rv = ld32(hsb + 512 + h * 4); \
      if (lead) ab = ld64(hsb + 256 + (lane & 31) * 8); \
    } }

  {
    float xv[8];
#pragma unroll
    for (int k = 0; k < 4; ++k) { xv[2 * k] = bflo(xr[k]); xv[2 * k + 1] = bfhi(xr[k]); }
    unsigned xn[4];
    ln_row(xv, xn);
    float acc[8] = {0.f, 0.f, 0.f, 0.f, 0.f, 0.f, 0.f, 0.f};
#pragma unroll
    for (int e = 0; e < 8; ++e) {
      const unsigned xp = (unsigned)__shfl((int)xn[e & 3], 8 * w + 2 * r + (e >> 2));
      mv2(A[2 * e], A[2 * e + 1], xp, acc);
    }
    float y[4];
#pragma unroll
    for (int k = 0; k < 4; ++k) y[k] = swapadd32(acc[k], acc[k + 4]);
    const float z0 = swapadd16(y[0], y[2]), z1 = swapadd16(y[1], y[3]);
    *reinterpret_cast<f32x2*>(lds + L_QP + (w * DH + 8 * c + 4 * (lane >> 5) + 2 * (r & 1)) * 4) = f32x2{z0, z1};
  }
  __syncthreads();
  if (tid < 64) {
    const f32x2* qp = reinterpret_cast<const f32x2*>(lds + L_QP);
    float a0 = 0.f, a1 = 0.f;
#pragma unroll
    for (int k = 0; k < NW; ++k) { const f32x2 t = qp[k * 64 + tid]; a0 += t[0]; a1 += t[1]; }
    reinterpret_cast<unsigned*>(lds + L_QF)[tid] = pk2(a0, a1);
  }
  __syncthreads();
  const u32x4 q = *reinterpret_cast<const u32x4*>(lds + L_QF + c * 16);

  STAMP(1);
  const u16* gub;
  {
    int cm = c < 2 * nch ? c : 2 * nch - 1;
    const bool up = cm >= nch;
    gub = (up ? wu : wg) + (size_t)(64 * w + 16 * r) * FF + (chunk0(h) + (up ? cm - nch : cm)) * 8;
  }
  float sc[16];
#pragma unroll
  for (int g = 0; g < 16; ++g) {
    float xs[8];
    const int sc0 = GOFF(g);
    const __amdgpu_buffer_rsrc_t rn = (g < 15) ? rK : rV;
    const int sn = (g < 15) ? GOFF(g + 1) : GOFF(0);
    const int l1 = opaque(lane);
    const u16* wob = wo + (size_t)(h * DH + (l1 >> 3)) * DM + 64 * w + 8 * (l1 & 7);
    if (g == 2) { HS_INIT }
    if (g >= 4 && (g & 1) == 0) { HS_STEP }
    SB();
#define KDOT(src) { float d = dot2(src[0], q[0], 0.f); d = dot2(src[1], q[1], d); d = dot2(src[2], q[2], d); d = dot2(src[3], q[3], d); asm volatile("" : "+v"(d)); xs[i_] = d; }
#define WOLD(i) { if (WO_PER == 16) { if (g == WO_G0) { A[(2 * (i)) & 15] = WLD(wob + ((2 * (i)) & 15) * 8 * DM); A[(2 * (i) + 1) & 15] = WLD(wob + ((2 * (i) + 1) & 15) * 8 * DM); } } \
      else if (g >= WO_G0 && (i) % (8 / (WO_PER > 8 ? 8 : WO_PER)) == 0 && (g - WO_G0) * WO_PER + (i) / (8 / (WO_PER > 8 ? 8 : WO_PER)) < 16) { \
        const int k_ = ((g - WO_G0) * WO_PER + (i) / (8 / (WO_PER > 8 ? 8 : WO_PER))) & 15; A[k_] = WLD(wob + k_ * 8 * DM); } }
#define KSTEP(i) { constexpr int i_ = i; \
      if (g == 1) { KDOT(X[i]) SB(); X[i] = WLD(gub + (i) * FF); } \
      else if (g == 2) { KDOT(X[8 + i]) SB(); X[8 + i] = WLD(gub + (8 + i) * FF); } \
      else { KDOT(R[i]) SB(); \
        if (g == 0) R[i] = ldkv<i>(rK, voff, GOFF(3)); \
        else if (i < 4) { if (g > 3) R[(i + 4) & 7] = ldkv<(i + 4) & 7>(rK, voff, sc0); } \
        else R[(i + 4) & 7] = ldkv<(i + 4) & 7>(rn, voff, sn); \
      } \
      WOLD(i) SB(); }
    KSTEP(0) KSTEP(1) KSTEP(2) KSTEP(3) KSTEP(4) KSTEP(5) KSTEP(6) KSTEP(7)
#undef KSTEP
#undef WOLD
#undef KDOT
#pragma unroll
    for (int k = 0; k < 4; ++k) {
      const float t1 = xs[k] + dpp<ROR8>(xs[k]), t2 = xs[k + 4] + dpp<ROR8>(xs[k + 4]);
      xs[k] = (c & 8) ? t2 : t1;
    }
#pragma unroll
    for (int k = 0; k < 2; ++k) {
      const float t1 = xs[k] + dpp<XOR7>(xs[k]), t2 = xs[k + 2] + dpp<XOR7>(xs[k + 2]);
      xs[k] = (c & 4) ? t2 : t1;
    }
    {
      const float t1 = xs[0] + dpp<XOR2>(xs[0]), t2 = xs[1] + dpp<XOR2>(xs[1]);
      float s = (c & 2) ? t2 : t1;
      s += dpp<XOR1>(s);
      sc[g] = rbf(rbf(s) * 0.08838834764831845f);
      asm volatile("" : "+v"(sc[g]));
    }
  }

  STAMP(2);
  {
    float m = sc[0];
#pragma unroll
    for (int g = 1; g < 16; ++g) m = fmaxf(m, sc[g]);
    m = wave_max(m);
    float l = 0.f;
#pragma unroll
    for (int g = 0; g < 16; ++g) l += expf(sc[g] - m);
    l = wave_sum(l) * 0.5f;
    if (lane == 0) *reinterpret_cast<f32x2*>(lds + L_ST + w * 8) = f32x2{m, l};
  }
  __syncthreads();
  {
    const f32x2 st = *reinterpret_cast<const f32x2*>(lds + L_ST + (c & 7) * 8);
    const float M = row_max(st[0]);
    const float L = row_sum(st[1] * expf(st[0] - M)) * 0.5f;
    if ((c & 1) == 0) {
#pragma unroll
      for (int g = 0; g < 16; ++g) {
        const __bf16 p = (__bf16)(expf(sc[g] - M) / L);
        *reinterpret_cast<u16*>(lds + L_P + ((w * 16 + g) * 4 + r) * 16 + (c >> 1) * 2) = bc<u16>(p);
      }
    }
  }

  STAMP(3);
  float acc[8] = {0.f, 0.f, 0.f, 0.f, 0.f, 0.f, 0.f, 0.f};
#pragma unroll
  for (int g = 0; g < 16; ++g) {
    const u32x4 pw = *reinterpret_cast<const u32x4*>(lds + L_P + ((w * 16 + g) * 4 + r) * 16);
    const int sc0 = GOFF(g), sn = GOFF(g + 1);
    if ((g & 3) == 1) { HS_STEP }
    SB();
#define VSTEP(ip) { mv2(R[2 * ip], R[2 * ip + 1], pw[ip], acc); \
      asm volatile("" : "+v"(acc[0]), "+v"(acc[1]), "+v"(acc[2]), "+v"(acc[3]), "+v"(acc[4]), "+v"(acc[5]), "+v"(acc[6]), "+v"(acc[7])); SB(); \
      if (ip < 2) { R[(2 * ip + 4) & 7] = ldkv<(2 * ip + 4) & 7>(rV, voff, sc0); R[(2 * ip + 5) & 7] = ldkv<(2 * ip + 5) & 7>(rV, voff, sc0); } \
      else if (g < 15) { R[(2 * ip + 4) & 7] = ldkv<(2 * ip + 4) & 7>(rV, voff, sn); R[(2 * ip + 5) & 7] = ldkv<(2 * ip + 5) & 7>(rV, voff, sn); } SB(); }
    VSTEP(0) VSTEP(1) VSTEP(2) VSTEP(3)
#undef VSTEP
  }
  STAMP(4);
  const int ln2 = opaque(lane), r2 = ln2 >> 4, c2 = ln2 & 15, td2 = w * 64 + ln2;
  const u32x4 xr2 = *reinterpret_cast<const u32x4*>(x + b * DM + ln2 * 8);
  {
    float y[4];
#pragma unroll
    for (int k = 0; k < 4; ++k) y[k] = swapadd32(acc[k], acc[k + 4]);
    const float z0 = swapadd16(y[0], y[2]), z1 = swapadd16(y[1], y[3]);
    *reinterpret_cast<f32x2*>(lds + L_QP + (w * DH + 8 * c2 + 4 * (ln2 >> 5) + 2 * (r2 & 1)) * 4) = f32x2{z0, z1};
  }
  for (unsigned spin = 0; w == 0 && !ready && spin < SPIN_CAP; ++spin) { HS_STEP __builtin_amdgcn_s_sleep(2); }
#undef HS_STEP
#undef HS_INIT
  __syncthreads();
  if (td2 < DH) {
    const float* op = reinterpret_cast<const float*>(lds + L_QP);
    float s = 0.f;
#pragma unroll
    for (int k = 0; k < NW; ++k) s += op[k * DH + td2];
    const __bf16 o = (__bf16)s;
    *reinterpret_cast<u16*>(lds + L_OF + ((td2 & 7) * 16 + (td2 >> 3)) * 2) = bc<u16>(o);
  }
  __syncthreads();
  unsigned char* const myslot = ws + WS_SLOT + (size_t)(b * NH + h) * 4096 + (64 * w + 8 * (ln2 & 7) + 4 * (ln2 >> 5) + 2 * (r2 & 1)) * 4;
  {
    const u32x4 ov0 = *reinterpret_cast<const u32x4*>(lds + L_OF + (ln2 >> 3) * 32);
    const u32x4 ov1 = *reinterpret_cast<const u32x4*>(lds + L_OF + (ln2 >> 3) * 32 + 16);
    float pa[8] = {0.f, 0.f, 0.f, 0.f, 0.f, 0.f, 0.f, 0.f};
#pragma unroll
    for (int e = 0; e < 16; ++e) pin(A[e]);
#pragma unroll
    for (int e = 0; e < 4; ++e) mv2(A[2 * e], A[2 * e + 1], ov0[e], pa);
#pragma unroll
    for (int e = 0; e < 4; ++e) mv2(A[8 + 2 * e], A[8 + 2 * e + 1], ov1[e], pa);
    float y[4];
#pragma unroll
    for (int k = 0; k < 4; ++k) y[k] = swapadd32(pa[k], pa[k + 4]);
    float z0 = swapadd16(y[0], y[2]), z1 = swapadd16(y[1], y[3]);
    z0 += dpp<ROR8>(z0); z1 += dpp<ROR8>(z1);
    if ((ln2 & 8) == 0) st_sc1_b64(myslot, z0, z1);
  }
  STAMP(5);

  const __amdgpu_buffer_rsrc_t rs = __builtin_amdgcn_make_buffer_rsrc((void*)(ws + WS_SLOT + (size_t)b * NH * 4096), 0, NH * 4096, 0x00020000);
  {
    const int off0 = r2 * 4096 + (64 * w + 4 * c2) * 4;
    u32x4 v[8];
    bool done[8] = {false, false, false, false, false, false, false, false};
    for (unsigned spin = 0;; ++spin) {
      asm volatile("" ::: "memory");
#pragma unroll
      for (int i = 0; i < 8; ++i) if (!done[i]) v[i] = __builtin_amdgcn_raw_buffer_load_b128(rs, off0 + i * 4 * 4096, 0, 16);
      bool all = true;
#pragma unroll
      for (int i = 0; i < 8; ++i) { if (!done[i]) done[i] = __all(v[i][0] != SENT && v[i][1] != SENT && v[i][2] != SENT && v[i][3] != SENT); all = all && done[i]; }
      if (all || spin > SPIN_CAP) break;
      __builtin_amdgcn_s_sleep(1);
    }
    STAMP(6);
    float s[4];
#pragma unroll
    for (int k = 0; k < 4; ++k) {
      s[k] = bc<float>(v[0][k]);
#pragma unroll
      for (int i = 1; i < 8; ++i) s[k] += bc<float>(v[i][k]);
    }
    const float y0 = swapadd32(s[0], s[2]), y1 = swapadd32(s[1], s[3]);
    const float z = swapadd16(y0, y1);
    reinterpret_cast<float*>(lds + L_HS)[64 * w + 4 * c2 + 2 * (ln2 >> 5) + (r2 & 1)] = z;
  }
  __syncthreads();
  {
    float hv[8];
    const f32x4* hs = reinterpret_cast<const f32x4*>(lds + L_HS);
    const f32x4 a0 = hs[2 * ln2], a1 = hs[2 * ln2 + 1];
#pragma unroll
    for (int k = 0; k < 4; ++k) {
      const float xa = (k & 1) ? bfhi(xr2[k >> 1]) : bflo(xr2[k >> 1]);
      const float xb = (k & 1) ? bfhi(xr2[2 + (k >> 1)]) : bflo(xr2[2 + (k >> 1)]);
      hv[k] = rbf(xa + rbf(a0[k]));
      hv[4 + k] = rbf(xb + rbf(a1[k]));
    }
    if (w == 0 && (ln2 >> 1) == h) {
      float* hr = reinterpret_cast<float*>(lds + L_HR) + (ln2 & 1) * 8;
#pragma unroll
      for (int k = 0; k < 8; ++k) hr[k] = hv[k];
    }
    unsigned hp[4];
    ln_row(hv, hp);
    float ga[8] = {0.f, 0.f, 0.f, 0.f, 0.f, 0.f, 0.f, 0.f};
#pragma unroll
    for (int e = 0; e < 16; ++e) pin(X[e]);
#pragma unroll
    for (int e = 0; e < 8; ++e) {
      const unsigned xp = (unsigned)__shfl((int)hp[e & 3], 8 * w + 2 * r2 + (e >> 2));
      mv2(X[2 * e], X[2 * e + 1], xp, ga);
    }
    float y[4];
#pragma unroll
    for (int k = 0; k < 4; ++k) y[k] = swapadd32(ga[k], ga[k + 4]);
    const float z0 = swapadd16(y[0], y[2]), z1 = swapadd16(y[1], y[3]);
    if (c2 < 2 * nch) *reinterpret_cast<f32x2*>(lds + L_GP + (w * 96 + c2 * 8 + 4 * (ln2 >> 5) + 2 * (r2 & 1)) * 4) = f32x2{z0, z1};
  }
  __syncthreads();
  {
    const float* gp = reinterpret_cast<const float*>(lds + L_GP);
    const bool on = ln2 < 8 * nch;
    const int li = on ? ln2 : 0;
    float g = 0.f, u = 0.f;
#pragma unroll
    for (int k = 0; k < NW; ++k) { g += gp[k * 96 + li]; u += gp[k * 96 + 8 * nch + li]; }
    g = rbf(g); u = rbf(u);
    const float sg = rbf(g / (1.f + expf(-g)));
    const float a = on ? rbf(sg * u) : 0.f;
    const unsigned ap = __builtin_amdgcn_perm(bc<unsigned>(dpp<ROR8>(a)), bc<unsigned>(a), 0x07060302u);
#pragma unroll
    for (int e = 0; e < 6; ++e) pin(DW[e]);
    if (nch == 5) DW[5] = u32x4{0u, 0u, 0u, 0u};
    float da[8] = {0.f, 0.f, 0.f, 0.f, 0.f, 0.f, 0.f, 0.f};
#pragma unroll
    for (int e = 0; e < 3; ++e) {
      const unsigned xp = (unsigned)__shfl((int)ap, 16 * e + (ln2 >> 3));
      mv2(DW[2 * e], DW[2 * e + 1], xp, da);
    }
    float y[4];
#pragma unroll
    for (int k = 0; k < 4; ++k) y[k] = swapadd32(da[k], da[k + 4]);
    float z0 = swapadd16(y[0], y[2]), z1 = swapadd16(y[1], y[3]);
    z0 += dpp<ROR8>(z0); z1 += dpp<ROR8>(z1);
    if ((ln2 & 8) == 0) st_sc1_b64(myslot + 2048, z0, z1);
  }
  STAMP(7);

  if (w == 0) {
    const int off0 = (ln2 >> 2) * 4096 + 2048 + (16 * h + 4 * (ln2 & 3)) * 4;
    u32x4 v0, v1;
    for (unsigned spin = 0;; ++spin) {
      asm volatile("" ::: "memory");
      v0 = __builtin_amdgcn_raw_buffer_load_b128(rs, off0, 0, 16);
      v1 = __builtin_amdgcn_raw_buffer_load_b128(rs, off0 + 16 * 4096, 0, 16);
      const bool ok = v0[0] != SENT && v0[1] != SENT && v0[2] != SENT && v0[3] != SENT && v1[0] != SENT && v1[1] != SENT && v1[2] != SENT && v1[3] != SENT;
      if (__all(ok) || spin > SPIN_CAP) break;
      __builtin_amdgcn_s_sleep(1);
    }
    STAMP(8);
    float s[4];
#pragma unroll
    for (int k = 0; k < 4; ++k) s[k] = bc<float>(v0[k]) + bc<float>(v1[k]);
    const float y0 = swapadd32(s[0], s[2]), y1 = swapadd32(s[1], s[3]);
    float z = swapadd16(y0, y1);
    z += dpp<ROR8>(z); z += dpp<ROR4>(z);
    if ((ln2 & 12) == 0) {
      const int col = 4 * (ln2 & 3) + 2 * (ln2 >> 5) + (r2 & 1);
      const float hres = reinterpret_cast<const float*>(lds + L_HR)[col];
      const __bf16 o = (__bf16)(hres + rbf(z));
      out[b * DM + 16 * h + col] = bc<u16>(o);
    }
  }
  STAMP(9);
  TSFLUSH;
#undef GOFF
}

}

extern "C" void kernel_launch(void* const* d_in, const int* in_sizes, int n_in,
                              void* d_out, int out_size, void* d_ws, size_t ws_size,
                              hipStream_t stream) {
  const u16* x = (const u16*)d_in[0];
  const u16* kc = (const u16*)d_in[1];
  const u16* vc = (const u16*)d_in[2];
  const u16* wq = (const u16*)d_in[3];
  const u16* wo = (const u16*)d_in[4];
  const u16* wg = (const u16*)d_in[6];
  const u16* wu = (const u16*)d_in[7];
  const u16* wd = (const u16*)d_in[8];
  u16* out = (u16*)d_out;
  unsigned char* ws = (unsigned char*)d_ws;
  hipLaunchKernelGGL(fused_kernel, dim3(NB * NH), dim3(NT), 0, stream, x, kc, vc, wq, wo, wg, wu, wd, out, ws);
}
```

```cpp
#include <hip/hip_runtime.h>
#include <hip/hip_bf16.h>
#include <stdint.h>

#define WLD(p) __builtin_nontemporal_load(reinterpret_cast<const u32x4*>(p))
#define KV_NT 1

#define STAMP(k)
#define TSARG
#define TSPASS
#define TSDECL
#define TSFLUSH

namespace {

constexpr int NB = 8, DM = 512, NH = 32, SK = 4096, DH = 128, FF = 1376;
constexpr int NW = 8, NT = NW * 64;
constexpr unsigned SENT = 0xFFFFFFFFu;
constexpr unsigned SPIN_CAP = 400000u;

using u16 = unsigned short;
using u32x4 = __attribute__((ext_vector_type(4))) unsigned;
using u32x2 = __attribute__((ext_vector_type(2))) unsigned;
using f32x4 = __attribute__((ext_vector_type(4))) float;
using f32x2 = __attribute__((ext_vector_type(2))) float;
using bf16x2 = __attribute__((ext_vector_type(2))) __bf16;

constexpr int S_P1 = 0;
constexpr int S_P2 = 2048;
constexpr int SLOT = 4096;
constexpr size_t WS_SLOT = 0;
constexpr size_t SLOT_BYTES = (size_t)NB * NH * SLOT;
constexpr size_t WS_HS = WS_SLOT + SLOT_BYTES;
constexpr int DEEP = 12;

constexpr int L_P = 0;
constexpr int L_QP = L_P + 8192;
constexpr int L_QF = L_QP + 4096;
constexpr int L_ST = L_QF + 256;
constexpr int L_OF = L_ST + 128;
constexpr int L_HR = L_OF + 2048;
constexpr int L_GP = L_HR + 64;
constexpr int L_END = L_GP + 8 * 96 * 4;

template <typename T, typename F> __device__ __forceinline__ T bc(F x) { return __builtin_bit_cast(T, x); }
__device__ __forceinline__ float rbf(float x) { return (float)(__bf16)x; }
__device__ __forceinline__ unsigned pk2(float lo, float hi) { bf16x2 v = {(__bf16)lo, (__bf16)hi}; return bc<unsigned>(v); }
__device__ __forceinline__ float bflo(unsigned u) { return bc<float>(u << 16); }
__device__ __forceinline__ float bfhi(unsigned u) { return bc<float>(u & 0xffff0000u); }
__device__ __forceinline__ float dot2(unsigned a, unsigned b, float c) {
  return __builtin_amdgcn_fdot2_f32_bf16(bc<bf16x2>(a), bc<bf16x2>(b), c, false);
}
template <int CTRL> __device__ __forceinline__ float dpp(float x) {
  return bc<float>(__builtin_amdgcn_mov_dpp(bc<int>(x), CTRL, 0xf, 0xf, true));
}
constexpr int XOR1 = 0xB1, XOR2 = 0x4E, XOR7 = 0x141, ROR8 = 0x128, ROR4 = 0x124;
__device__ __forceinline__ float swapadd32(float a, float b) {
  auto s = __builtin_amdgcn_permlane32_swap(bc<unsigned>(a), bc<unsigned>(b), false, false);
  return bc<float>(s[0]) + bc<float>(s[1]);
}
__device__ __forceinline__ float swapadd16(float a, float b) {
  auto s = __builtin_amdgcn_permlane16_swap(bc<unsigned>(a), bc<unsigned>(b), false, false);
  return bc<float>(s[0]) + bc<float>(s[1]);
}
__device__ __forceinline__ float swapmax32(float a) {
  auto s = __builtin_amdgcn_permlane32_swap(bc<unsigned>(a), bc<unsigned>(a), false, false);
  return fmaxf(bc<float>(s[0]), bc<float>(s[1]));
}
__device__ __forceinline__ float swapmax16(float a) {
  auto s = __builtin_amdgcn_permlane16_swap(bc<unsigned>(a), bc<unsigned>(a), false, false);
  return fmaxf(bc<float>(s[0]), bc<float>(s[1]));
}
__device__ __forceinline__ float row_sum(float x) {
  x += dpp<XOR1>(x); x += dpp<XOR2>(x); x += dpp<XOR7>(x); x += dpp<ROR8>(x); return x;
}
__device__ __forceinline__ float row_max(float x) {
  x = fmaxf(x, dpp<XOR1>(x)); x = fmaxf(x, dpp<XOR2>(x)); x = fmaxf(x, dpp<XOR7>(x)); x = fmaxf(x, dpp<ROR8>(x)); return x;
}
__device__ __forceinline__ float wave_sum(float x) { x = row_sum(x); x = swapadd16(x, x); return swapadd32(x, x); }
__device__ __forceinline__ float wave_max(float x) { x = row_max(x); x = swapmax16(x); return swapmax32(x); }

__device__ __forceinline__ void mv2(const u32x4& w0, const u32x4& w1, unsigned xp, float (&acc)[8]) {
#pragma unroll
  for (int k = 0; k < 4; ++k) {
    const unsigned lo = __builtin_amdgcn_perm(w1[k], w0[k], 0x05040100u);
    const unsigned hi = __builtin_amdgcn_perm(w1[k], w0[k], 0x07060302u);
    acc[2 * k] = dot2(lo, xp, acc[2 * k]);
    acc[2 * k + 1] = dot2(hi, xp, acc[2 * k + 1]);
  }
}

__device__ __forceinline__ void ln_row(const float (&v)[8], unsigned (&o)[4]) {
  float s = 0.f;
#pragma unroll
  for (int k = 0; k < 8; ++k) s += v[k];
  const float mean = wave_sum(s) * (1.f / DM);
  float q = 0.f;
#pragma unroll
  for (int k = 0; k < 8; ++k) { const float d = v[k] - mean; q = fmaf(d, d, q); }
  const float var = wave_sum(q) * (1.f / DM);
  const float mb = rbf(mean);
  const float den = rbf(sqrtf(rbf(rbf(var) + 1e-5f)));
#pragma unroll
  for (int k = 0; k < 4; ++k) o[k] = pk2(rbf(v[2 * k] - mb) / den, rbf(v[2 * k + 1] - mb) / den);
}

template <int I>
__device__ __forceinline__ u32x4 ldkv(__amdgpu_buffer_rsrc_t rs, int voff, int soff) {
  return __builtin_amdgcn_raw_buffer_load_b128(rs, voff + (I & 3) * 1024, soff + (I >> 2) * 4096, KV_NT ? 2 : 0);
}
__device__ __forceinline__ int opaque(int v) { asm volatile("" : "+v"(v)); return v; }
#define SB() __builtin_amdgcn_sched_barrier(0)

__device__ __forceinline__ void st_sc1_b64(void* p, float a, float b) {
  unsigned ua = bc<unsigned>(a), ub = bc<unsigned>(b);
  ua = (ua == SENT) ? 0x7FC00000u : ua; ub = (ub == SENT) ? 0x7FC00000u : ub;
  __hip_atomic_store(reinterpret_cast<unsigned long long*>(p), ((unsigned long long)ub << 32) | ua, __ATOMIC_RELAXED, __HIP_MEMORY_SCOPE_AGENT);
}

__device__ __forceinline__ int chunk0(int j) { return 5 * j + (j < 12 ? j : 12); }

__device__ __forceinline__ void pin(u32x4& v) { asm volatile("" : "+v"(v)); }
__device__ __forceinline__ unsigned ld32(const void* p) { return __hip_atomic_load(reinterpret_cast<const unsigned*>(p), __ATOMIC_RELAXED, __HIP_MEMORY_SCOPE_AGENT); }
__device__ __forceinline__ unsigned long long ld64(const void* p) { return __hip_atomic_load(reinterpret_cast<const unsigned long long*>(p), __ATOMIC_RELAXED, __HIP_MEMORY_SCOPE_AGENT); }
__device__ __forceinline__ void st32(void* p, unsigned v) { __hip_atomic_store(reinterpret_cast<unsigned*>(p), v, __ATOMIC_RELAXED, __HIP_MEMORY_SCOPE_AGENT); }
__device__ __forceinline__ void st64(void* p, unsigned long long v) { __hip_atomic_store(reinterpret_cast<unsigned long long*>(p), v, __ATOMIC_RELAXED, __HIP_MEMORY_SCOPE_AGENT); }
__device__ __forceinline__ unsigned rfl(unsigned v) { return (unsigned)__builtin_amdgcn_readfirstlane((int)v); }

__device__ __forceinline__ bool nosent(const u32x4& v) { return v[0] != SENT && v[1] != SENT && v[2] != SENT && v[3] != SENT; }
__device__ __forceinline__ int rot_of(int b, int h) { return 16 * (h >> 2) + 2 * b; }

__global__ void __launch_bounds__(NT, 2)
fused_kernel(const u16* __restrict__ x, const u16* __restrict__ kc, const u16* __restrict__ vc, const u16* __restrict__ wq,
             const u16* __restrict__ wo, const u16* __restrict__ wg, const u16* __restrict__ wu, const u16* __restrict__ wd,
             u16* __restrict__ out, unsigned char* __restrict__ ws) {
  __shared__ __attribute__((aligned(16))) unsigned char lds[L_END];
  const int tid = threadIdx.x, lane = tid & 63;
  const int w = __builtin_amdgcn_readfirstlane(tid >> 6);
  const int r = lane >> 4, c = lane & 15;
  const int bid = blockIdx.x;
  const int h = (bid & 7) * 4 + ((bid >> 3) & 3), b = bid >> 5;
  int rot8 = rot_of(b, h) + w;
  const int nch = h < 12 ? 6 : 5;

  const __amdgpu_buffer_rsrc_t rK = __builtin_amdgcn_make_buffer_rsrc((void*)(kc + (size_t)(b * NH + h) * SK * DH), 0, SK * DH * 2, 0x00020000);
  const __amdgpu_buffer_rsrc_t rV = __builtin_amdgcn_make_buffer_rsrc((void*)(vc + (size_t)(b * NH + h) * SK * DH), 0, SK * DH * 2, 0x00020000);
  const __amdgpu_buffer_rsrc_t rs = __builtin_amdgcn_make_buffer_rsrc((void*)(ws + WS_SLOT + (size_t)b * NH * SLOT), 0, NH * SLOT, 0x00020000);
  unsigned char* const slotb = ws + WS_SLOT + (size_t)b * NH * SLOT;
  const int voff = lane * 16;
#define GOFF(g) (((((g) & 15) * 8 + rot8) & 127) * 8192)

  TSDECL
  STAMP(0);
  unsigned char* const hsb = ws + WS_HS + (size_t)b * 1024;
  const bool lead = h == 0;
  if (tid < 256) __builtin_amdgcn_raw_buffer_store_b128(u32x4{SENT, SENT, SENT, SENT}, rs, tid * 16, h * SLOT, 16);
  unsigned rv = ld32(hsb + 512 + h * 4), gv = 0u;
  unsigned long long ab = ld64(hsb + 256 + (lane & 31) * 8);
  SB();
  const u32x4 xr = *reinterpret_cast<const u32x4*>(x + b * DM + lane * 8);
  u32x4 A[16], X[16], R[8], DW[6];
  {
    const u16* base = wq + (size_t)(64 * w + 16 * r) * (NH * DH) + h * DH + 8 * c;
#define IL(e) A[e] = WLD(base + (e) * (NH * DH)); SB(); R[e] = ldkv<e>(rK, voff, GOFF(0)); SB();
    IL(0) IL(1) IL(2) IL(3) IL(4) IL(5) IL(6) IL(7)
#undef IL
#define IL(e) A[8 + e] = WLD(base + (8 + e) * (NH * DH)); SB(); X[e] = ldkv<e>(rK, voff, GOFF(1)); SB();
    IL(0) IL(1) IL(2) IL(3) IL(4) IL(5) IL(6) IL(7)
#undef IL
  }
  SB();
#define LD8(dst, o, rs_, so) dst[o + 0] = ldkv<0>(rs_, voff, so); dst[o + 1] = ldkv<1>(rs_, voff, so); dst[o + 2] = ldkv<2>(rs_, voff, so); dst[o + 3] = ldkv<3>(rs_, voff, so); \
                             dst[o + 4] = ldkv<4>(rs_, voff, so); dst[o + 5] = ldkv<5>(rs_, voff, so); dst[o + 6] = ldkv<6>(rs_, voff, so); dst[o + 7] = ldkv<7>(rs_, voff, so);
  LD8(X, 8, rK, GOFF(2))
  SB();
  const unsigned r1 = rfl(rv) + 1u;
  unsigned tagN = 0u, eg = 0u;
  bool ech = false, granted = false, ready = false;
  if (lead && w == 0) {
    while (__any((unsigned)ab == tagN)) ++tagN;
    if (lane == 0) st32(hsb, tagN);
  }
#define HS_INIT if (w == 0) { gv = ld32(hsb); rv = ld32(hsb + 512 + h * 4); }
#define HS_STEP if (w == 0 && !ready) { \
    const unsigned g_ = rfl(gv), r_ = rfl(rv); \
    if (r_ == r1) ready = true; \
    else { \
      if (!ech || g_ != eg) { if (lane == 0) st64(hsb + 256 + h * 8, (unsigned long long)g_ | ((unsigned long long)r1 << 32)); eg = g_; ech = true; } \
      if (lead && !granted) { if (__all((unsigned)ab == tagN)) { if (lane < 32) st32(hsb + 512 + lane * 4, (unsigned)(ab >> 32)); granted = true; } } \
      gv = ld32(hsb); rv = ld32(hsb + 512 + h * 4); \
      if (lead) ab = ld64(hsb + 256 + (lane & 31) * 8); \
    } }
#define ROWRED(av, z0, z1) float z0, z1; { float y_[4]; \
    _Pragma("unroll") for (int k = 0; k < 4; ++k) y_[k] = swapadd32(av[k], av[k + 4]); \
    z0 = swapadd16(y_[0], y_[2]); z1 = swapadd16(y_[1], y_[3]); }

  {
    float xv[8];
#pragma unroll
    for (int k = 0; k < 4; ++k) { xv[2 * k] = bflo(xr[k]); xv[2 * k + 1] = bfhi(xr[k]); }
    unsigned xn[4];
    ln_row(xv, xn);
    float acc[8] = {0.f, 0.f, 0.f, 0.f, 0.f, 0.f, 0.f, 0.f};
#pragma unroll
    for (int e = 0; e < 8; ++e) {
      const unsigned xp = (unsigned)__shfl((int)xn[e & 3], 8 * w + 2 * r + (e >> 2));
      mv2(A[2 * e], A[2 * e + 1], xp, acc);
    }
    ROWRED(acc, z0, z1)
    *reinterpret_cast<f32x2*>(lds + L_QP + (w * DH + 8 * c + 4 * (lane >> 5) + 2 * (r & 1)) * 4) = f32x2{z0, z1};
  }
  __syncthreads();
  if (tid < 64) {
    const f32x2* qp = reinterpret_cast<const f32x2*>(lds + L_QP);
    float a0 = 0.f, a1 = 0.f;
#pragma unroll
    for (int k = 0; k < NW; ++k) { const f32x2 t = qp[k * 64 + tid]; a0 += t[0]; a1 += t[1]; }
    reinterpret_cast<unsigned*>(lds + L_QF)[tid] = pk2(a0, a1);
  }
  __syncthreads();
  const u32x4 q = *reinterpret_cast<const u32x4*>(lds + L_QF + c * 16);

  STAMP(1);
  const u16* gub;
  {
    int cm = c < 2 * nch ? c : 2 * nch - 1;
    const bool up = cm >= nch;
    gub = (up ? wu : wg) + (size_t)(64 * w + r) * FF + (chunk0(h) + (up ? cm - nch : cm)) * 8;
  }
  float mx = -INFINITY;
  unsigned char* const psc = lds + L_P + (w * 64 + r) * 16 + (c >> 1) * 2;
#pragma unroll
  for (int g = 0; g < 16; ++g) {
    float xs[8];
    const int sc0 = GOFF(g), sn = GOFF(g + 1);
    const __amdgpu_buffer_rsrc_t rn = (g < 15) ? rK : rV;
    const int l1 = opaque(lane);
    const u16* wdb = wd + (size_t)(chunk0(h) * 8 + (l1 >> 3)) * DM + 64 * w + 8 * (l1 & 7);
    if (g == 3) { HS_INIT }
    if (g > 3 && (g & 1)) { HS_STEP }
    SB();
#define KDOT(src) { float d = dot2(src[0], q[0], 0.f); d = dot2(src[1], q[1], d); d = dot2(src[2], q[2], d); d = dot2(src[3], q[3], d); asm volatile("" : "+v"(d)); xs[i_] = d; }
#define KSTEP(i) { constexpr int i_ = i; \
      if (g == 1) { KDOT(X[i]) SB(); } \
      else if (g == 2) { KDOT(X[8 + i]) SB(); } \
      else { KDOT(R[i]) SB(); \
        if (g == 0) R[i] = ldkv<i>(rK, voff, GOFF(3)); \
        else if (i >= 4) R[(i + 4) & 7] = ldkv<(i + 4) & 7>(rn, voff, sn); \
        else if (g > 3) R[(i + 4) & 7] = ldkv<(i + 4) & 7>(rK, voff, sc0); \
      } \
      if (g >= 3 && g < 14 && ((i) & 3) == 1) { const int k_ = (g - 3) * 2 + ((i) >> 2); \
        if (k_ < 16) X[k_ & 15] = WLD(gub + (k_ & 15) * 4 * FF); \
        else { const int d_ = (k_ - 16) % 6; DW[d_] = WLD(wdb + (d_ < 5 || nch == 6 ? d_ : 0) * 8 * DM); } } \
      SB(); }
    KSTEP(0) KSTEP(1) KSTEP(2) KSTEP(3) KSTEP(4) KSTEP(5) KSTEP(6) KSTEP(7)
#undef KSTEP
#undef KDOT
#pragma unroll
    for (int k = 0; k < 4; ++k) {
      const float t1 = xs[k] + dpp<ROR8>(xs[k]), t2 = xs[k + 4] + dpp<ROR8>(xs[k + 4]);
      xs[k] = (c & 8) ? t2 : t1;
    }
#pragma unroll
    for (int k = 0; k < 2; ++k) {
      const float t1 = xs[k] + dpp<XOR7>(xs[k]), t2 = xs[k + 2] + dpp<XOR7>(xs[k + 2]);
      xs[k] = (c & 4) ? t2 : t1;
    }
    const float t1 = xs[0] + dpp<XOR2>(xs[0]), t2 = xs[1] + dpp<XOR2>(xs[1]);
    float s = (c & 2) ? t2 : t1;
    s += dpp<XOR1>(s);
    const __bf16 sb = (__bf16)(rbf(s) * 0.08838834764831845f);
    mx = fmaxf(mx, (float)sb);
    if ((c & 1) == 0) *reinterpret_cast<u16*>(psc + g * 64) = bc<u16>(sb);
  }

  STAMP(2);
  {
    u32x4* const pcell = reinterpret_cast<u32x4*>(lds + L_P + (w * 64 + lane) * 16);
    const u32x4 sv = *pcell;
    const float m = wave_max(mx);
    float l = 0.f;
#pragma unroll
    for (int k = 0; k < 4; ++k) l += expf(bflo(sv[k]) - m) + expf(bfhi(sv[k]) - m);
    l = wave_sum(l);
    if (lane == 0) *reinterpret_cast<f32x2*>(lds + L_ST + w * 8) = f32x2{m, l};
    __syncthreads();
    const f32x2 st = *reinterpret_cast<const f32x2*>(lds + L_ST + (c & 7) * 8);
    const float M = row_max(st[0]);
    const float L = row_sum(st[1] * expf(st[0] - M)) * 0.5f;
    u32x4 pv;
#pragma unroll
    for (int k = 0; k < 4; ++k) pv[k] = pk2(expf(bflo(sv[k]) - M) / L, expf(bfhi(sv[k]) - M) / L);
    *pcell = pv;
  }

  STAMP(3);
  float acc[8] = {0.f, 0.f, 0.f, 0.f, 0.f, 0.f, 0.f, 0.f};
  asm volatile("" : "+s"(rot8));
#define VMAC(ip) mv2(R[2 * ip], R[2 * ip + 1], pw[ip], acc); \
      asm volatile("" : "+v"(acc[0]), "+v"(acc[1]), "+v"(acc[2]), "+v"(acc[3]), "+v"(acc[4]), "+v"(acc[5]), "+v"(acc[6]), "+v"(acc[7])); SB();
#define VLD(i, so) R[i] = ldkv<i>(rV, voff, so); R[i + 1] = ldkv<i + 1>(rV, voff, so);
#pragma unroll
  for (int g = 0; g < 16; ++g) {
    const u32x4 pw = *reinterpret_cast<const u32x4*>(lds + L_P + ((w * 16 + g) * 4 + r) * 16);
    const int sc0 = GOFF(g), sn = GOFF(g + 1);
    const int l1 = opaque(lane);
    const u16* wob = wo + (size_t)(h * DH + (l1 >> 3)) * DM + 64 * w + 8 * (l1 & 7);
    if ((g & 3) == 1) { HS_STEP }
    SB();
    if (g < DEEP) {
      VMAC(0) VLD(4, sc0) if (g < 8) A[2 * (g & 7)] = WLD(wob + 2 * (g & 7) * 8 * DM); SB();
      VMAC(1) VLD(6, sc0) SB();
      VMAC(2) if (g < 15) { VLD(0, sn) } if (g < 8) A[2 * (g & 7) + 1] = WLD(wob + (2 * (g & 7) + 1) * 8 * DM); SB();
      VMAC(3) if (g < 15) { VLD(2, sn) } SB();
      if (g == DEEP - 1 && g < 15) { VLD(4, sn) VLD(6, sn) SB(); }
    } else {
      VMAC(0) if (g < 15) { VLD(0, sn) } SB();
      VMAC(1) if (g < 15) { VLD(2, sn) } SB();
      VMAC(2) if (g < 15) { VLD(4, sn) } SB();
      VMAC(3) if (g < 15) { VLD(6, sn) } SB();
    }
  }
#undef VLD
#undef VMAC
  STAMP(4);
  const int ln2 = opaque(lane), r2 = ln2 >> 4, c2 = ln2 & 15;
  const int hcol = 64 * w + 4 * c2 + 2 * (ln2 >> 5) + (r2 & 1);
  const unsigned xcol = x[b * DM + hcol];
  {
    ROWRED(acc, z0, z1)
    *reinterpret_cast<f32x2*>(lds + L_QP + (w * DH + 8 * c2 + 4 * (ln2 >> 5) + 2 * (r2 & 1)) * 4) = f32x2{z0, z1};
  }
  for (unsigned spin = 0; w == 0 && !ready && spin < SPIN_CAP; ++spin) { HS_STEP __builtin_amdgcn_s_sleep(2); }
#undef HS_STEP
#undef HS_INIT
  __syncthreads();
  STAMP(10);
  {
    const int d = (ln2 >> 3) * 16 + (ln2 & 7);
    const float* op = reinterpret_cast<const float*>(lds + L_QP) + d;
    float s0 = 0.f, s1 = 0.f;
#pragma unroll
    for (int k = 0; k < NW; ++k) { s0 += op[k * DH]; s1 += op[k * DH + 8]; }
    *reinterpret_cast<unsigned*>(lds + L_OF + w * 256 + (ln2 & 7) * 32 + (ln2 >> 3) * 4) = pk2(s0, s1);
    __builtin_amdgcn_wave_barrier();
  }
  STAMP(11);
  unsigned char* const myslot = slotb + h * SLOT + (64 * w + 8 * (ln2 & 7) + 4 * (ln2 >> 5) + 2 * (r2 & 1)) * 4;
  {
    const u32x4 ov0 = *reinterpret_cast<const u32x4*>(lds + L_OF + w * 256 + (ln2 >> 3) * 32);
    const u32x4 ov1 = *reinterpret_cast<const u32x4*>(lds + L_OF + w * 256 + (ln2 >> 3) * 32 + 16);
    float pa[8] = {0.f, 0.f, 0.f, 0.f, 0.f, 0.f, 0.f, 0.f};
#pragma unroll
    for (int e = 0; e < 16; ++e) pin(A[e]);
#pragma unroll
    for (int e = 0; e < 4; ++e) mv2(A[2 * e], A[2 * e + 1], ov0[e], pa);
#pragma unroll
    for (int e = 0; e < 4; ++e) mv2(A[8 + 2 * e], A[8 + 2 * e + 1], ov1[e], pa);
    ROWRED(pa, z0, z1)
    z0 += dpp<ROR8>(z0); z1 += dpp<ROR8>(z1);
    if ((ln2 & 8) == 0) st_sc1_b64(myslot + S_P1, z0, z1);
  }
  STAMP(5);

#undef LD8
  STAMP(12);

  float hv;
  {
    const int off0 = r2 * SLOT + S_P1 + (64 * w + 4 * c2) * 4;
    u32x4 v[8];
    bool done[8] = {false, false, false, false, false, false, false, false};
    for (unsigned spin = 0;; ++spin) {
      asm volatile("" ::: "memory");
#pragma unroll
      for (int i = 0; i < 8; ++i) if (!done[i]) v[i] = __builtin_amdgcn_raw_buffer_load_b128(rs, off0 + i * 4 * SLOT, 0, 16);
      bool all = true;
#pragma unroll
      for (int i = 0; i < 8; ++i) { if (!done[i]) done[i] = __all(nosent(v[i])); all = all && done[i]; }
      if (all || spin > SPIN_CAP) break;
      __builtin_amdgcn_s_sleep(1);
    }
    STAMP(6);
    float s[4];
#pragma unroll
    for (int k = 0; k < 4; ++k) {
      s[k] = bc<float>(v[0][k]);
#pragma unroll
      for (int i = 1; i < 8; ++i) s[k] += bc<float>(v[i][k]);
    }
    const float y0 = swapadd32(s[0], s[2]), y1 = swapadd32(s[1], s[3]);
    hv = rbf(bflo(xcol) + rbf(swapadd16(y0, y1)));
    if ((hcol >> 4) == h) reinterpret_cast<float*>(lds + L_HR)[hcol & 15] = hv;
    const float mw = wave_sum(hv) * (1.f / 64.f);
    const float m2 = wave_sum((hv - mw) * (hv - mw));
    if (ln2 == 0) *reinterpret_cast<f32x2*>(lds + L_ST + 64 + w * 8) = f32x2{mw, m2};
  }
  __syncthreads();
  STAMP(13);
  {
    const f32x2 st = *reinterpret_cast<const f32x2*>(lds + L_ST + 64 + (c2 & 7) * 8);
    const float mean = row_sum(st[0]) * (0.5f / 8.f);
    const float var = row_sum(fmaf(64.f * (st[0] - mean), st[0] - mean, st[1])) * (0.5f / DM);
    const float den = rbf(sqrtf(rbf(rbf(var) + 1e-5f)));
    const float hn = rbf(rbf(hv - rbf(mean)) / den);
    const unsigned hp = __builtin_amdgcn_perm(bc<unsigned>(dpp<XOR1>(hn)), bc<unsigned>(hn), 0x07060302u);
    float ga[8] = {0.f, 0.f, 0.f, 0.f, 0.f, 0.f, 0.f, 0.f};
#pragma unroll
    for (int e = 0; e < 16; ++e) pin(X[e]);
#define GU(e) mv2(X[2 * e], X[2 * e + 1], (unsigned)__builtin_amdgcn_mov_dpp((int)hp, 0x150 + 2 * e, 0xf, 0xf, true), ga);
    GU(0) GU(1) GU(2) GU(3) GU(4) GU(5) GU(6) GU(7)
#undef GU
    ROWRED(ga, z0, z1)
    if (c2 < 2 * nch) *reinterpret_cast<f32x2*>(lds + L_GP + (w * 96 + c2 * 8 + 4 * (ln2 >> 5) + 2 * (r2 & 1)) * 4) = f32x2{z0, z1};
  }
  STAMP(14);
  __syncthreads();
  STAMP(15);
  {
    const float* gp = reinterpret_cast<const float*>(lds + L_GP);
    const bool on = ln2 < 8 * nch;
    const int li = on ? ln2 : 0;
    float g = 0.f, u = 0.f;
#pragma unroll
    for (int k = 0; k < NW; ++k) { g += gp[k * 96 + li]; u += gp[k * 96 + 8 * nch + li]; }
    g = rbf(g); u = rbf(u);
    const float sg = rbf(g / (1.f + expf(-g)));
    const float a = on ? rbf(sg * u) : 0.f;
    const unsigned ap = __builtin_amdgcn_perm(bc<unsigned>(dpp<ROR8>(a)), bc<unsigned>(a), 0x07060302u);
#pragma unroll
    for (int e = 0; e < 6; ++e) pin(DW[e]);
    if (nch == 5) DW[5] = u32x4{0u, 0u, 0u, 0u};
    float da[8] = {0.f, 0.f, 0.f, 0.f, 0.f, 0.f, 0.f, 0.f};
#pragma unroll
    for (int e = 0; e < 3; ++e) {
      const unsigned xp = (unsigned)__shfl((int)ap, 16 * e + (ln2 >> 3));
      mv2(DW[2 * e], DW[2 * e + 1], xp, da);
    }
    ROWRED(da, z0, z1)
    z0 += dpp<ROR8>(z0); z1 += dpp<ROR8>(z1);
    if ((ln2 & 8) == 0) st_sc1_b64(myslot + S_P2, z0, z1);
  }
  STAMP(7);

  if (w == 0) {
    const int off0 = (ln2 >> 2) * SLOT + S_P2 + (16 * h + 4 * (ln2 & 3)) * 4;
    u32x4 v0, v1;
    for (unsigned spin = 0;; ++spin) {
      asm volatile("" ::: "memory");
      v0 = __builtin_amdgcn_raw_buffer_load_b128(rs, off0, 0, 16);
      v1 = __builtin_amdgcn_raw_buffer_load_b128(rs, off0 + 16 * SLOT, 0, 16);
      if (__all(nosent(v0) && nosent(v1)) || spin > SPIN_CAP) break;
      __builtin_amdgcn_s_sleep(1);
    }
    STAMP(8);
    float s[4];
#pragma unroll
    for (int k = 0; k < 4; ++k) s[k] = bc<float>(v0[k]) + bc<float>(v1[k]);
    const float y0 = swapadd32(s[0], s[2]), y1 = swapadd32(s[1], s[3]);
    float z = swapadd16(y0, y1);
    z += dpp<ROR8>(z); z += dpp<ROR4>(z);
    if ((ln2 & 12) == 0) {
      const int col = 4 * (ln2 & 3) + 2 * (ln2 >> 5) + (r2 & 1);
      const float hres = reinterpret_cast<const float*>(lds + L_HR)[col];
      const __bf16 o = (__bf16)(hres + rbf(z));
      out[b * DM + 16 * h + col] = bc<u16>(o);
    }
  }
  STAMP(9);
  TSFLUSH;
#undef GOFF
#undef ROWRED
}

}

extern "C" void kernel_launch(void* const* d_in, const int* in_sizes, int n_in,
                              void* d_out, int out_size, void* d_ws, size_t ws_size,
                              hipStream_t stream) {
  const u16* x = (const u16*)d_in[0];
  const u16* kc = (const u16*)d_in[1];
  const u16* vc = (const u16*)d_in[2];
  const u16* wq = (const u16*)d_in[3];
  const u16* wo = (const u16*)d_in[4];
  const u16* wg = (const u16*)d_in[6];
  const u16* wu = (const u16*)d_in[7];
  const u16* wd = (const u16*)d_in[8];
  u16* out = (u16*)d_out;
  unsigned char* ws = (unsigned char*)d_ws;
  hipLaunchKernelGGL(fused_kernel, dim3(NB * NH), dim3(NT), 0, stream, x, kc, vc, wq, wo, wg, wu, wd, out, ws);
}
```

```cpp
#include <hip/hip_runtime.h>
#include <hip/hip_bf16.h>
#include <stdint.h>

#define WLD(p) __builtin_nontemporal_load(reinterpret_cast<const u32x4*>(p))
#define KV_NT 1

#define STAMP(k)
#define TSARG
#define TSPASS
#define TSDECL
#define TSFLUSH

namespace {

constexpr int NB = 8, DM = 512, NH = 32, SK = 4096, DH = 128, FF = 1376;
constexpr int NW = 8, NT = NW * 64;
constexpr unsigned SENT = 0xFFFFFFFFu;
constexpr unsigned SPIN_CAP = 400000u;

using u16 = unsigned short;
using u32x4 = __attribute__((ext_vector_type(4))) unsigned;
using u32x2 = __attribute__((ext_vector_type(2))) unsigned;
using f32x4 = __attribute__((ext_vector_type(4))) float;
using f32x2 = __attribute__((ext_vector_type(2))) float;
using bf16x2 = __attribute__((ext_vector_type(2))) __bf16;

constexpr int S_P1 = 0;
constexpr int S_P2 = 2048;
constexpr int SLOT = 4096;
constexpr size_t WS_SLOT = 0;
constexpr size_t SLOT_BYTES = (size_t)NB * NH * SLOT;
constexpr size_t WS_HS = WS_SLOT + SLOT_BYTES;
constexpr int DEEP = 12;

constexpr int L_P = 0;
constexpr int L_QP = L_P + 8192;
constexpr int L_QF = L_QP + 4096;
constexpr int L_ST = L_QF + 256;
constexpr int L_OF = L_ST + 128;
constexpr int L_HR = L_OF + 2048;
constexpr int L_GP = L_HR + 64;
constexpr int L_END = L_GP + 8 * 96 * 4;

template <typename T, typename F> __device__ __forceinline__ T bc(F x) { return __builtin_bit_cast(T, x); }
__device__ __forceinline__ float rbf(float x) { return (float)(__bf16)x; }
__device__ __forceinline__ unsigned pk2(float lo, float hi) { bf16x2 v = {(__bf16)lo, (__bf16)hi}; return bc<unsigned>(v); }
__device__ __forceinline__ float bflo(unsigned u) { return bc<float>(u << 16); }
__device__ __forceinline__ float bfhi(unsigned u) { return bc<float>(u & 0xffff0000u); }
__device__ __forceinline__ float dot2(unsigned a, unsigned b, float c) {
  return __builtin_amdgcn_fdot2_f32_bf16(bc<bf16x2>(a), bc<bf16x2>(b), c, false);
}
template <int CTRL> __device__ __forceinline__ float dpp(float x) {
  return bc<float>(__builtin_amdgcn_mov_dpp(bc<int>(x), CTRL, 0xf, 0xf, true));
}
constexpr int XOR1 = 0xB1, XOR2 = 0x4E, XOR7 = 0x141, ROR8 = 0x128, ROR4 = 0x124;
__device__ __forceinline__ float swapadd32(float a, float b) {
  auto s = __builtin_amdgcn_permlane32_swap(bc<unsigned>(a), bc<unsigned>(b), false, false);
  return bc<float>(s[0]) + bc<float>(s[1]);
}
__device__ __forceinline__ float swapadd16(float a, float b) {
  auto s = __builtin_amdgcn_permlane16_swap(bc<unsigned>(a), bc<unsigned>(b), false, false);
  return bc<float>(s[0]) + bc<float>(s[1]);
}
__device__ __forceinline__ float swapmax32(float a) {
  auto s = __builtin_amdgcn_permlane32_swap(bc<unsigned>(a), bc<unsigned>(a), false, false);
  return fmaxf(bc<float>(s[0]), bc<float>(s[1]));
}
__device__ __forceinline__ float swapmax16(float a) {
  auto s = __builtin_amdgcn_permlane16_swap(bc<unsigned>(a), bc<unsigned>(a), false, false);
  return fmaxf(bc<float>(s[0]), bc<float>(s[1]));
}
__device__ __forceinline__ float row_sum(float x) {
  x += dpp<XOR1>(x); x += dpp<XOR2>(x); x += dpp<XOR7>(x); x += dpp<ROR8>(x); return x;
}
__device__ __forceinline__ float row_max(float x) {
  x = fmaxf(x, dpp<XOR1>(x)); x = fmaxf(x, dpp<XOR2>(x)); x = fmaxf(x, dpp<XOR7>(x)); x = fmaxf(x, dpp<ROR8>(x)); return x;
}
__device__ __forceinline__ float wave_sum(float x) { x = row_sum(x); x = swapadd16(x, x); return swapadd32(x, x); }
__device__ __forceinline__ float wave_max(float x) { x = row_max(x); x = swapmax16(x); return swapmax32(x); }

__device__ __forceinline__ void mv2(const u32x4& w0, const u32x4& w1, unsigned xp, float (&acc)[8]) {
#pragma unroll
  for (int k = 0; k < 4; ++k) {
    const unsigned lo = __builtin_amdgcn_perm(w1[k], w0[k], 0x05040100u);
    const unsigned hi = __builtin_amdgcn_perm(w1[k], w0[k], 0x07060302u);
    acc[2 * k] = dot2(lo, xp, acc[2 * k]);
    acc[2 * k + 1] = dot2(hi, xp, acc[2 * k + 1]);
  }
}

__device__ __forceinline__ void ln_row(const float (&v)[8], unsigned (&o)[4]) {
  float s = 0.f;
#pragma unroll
  for (int k = 0; k < 8; ++k) s += v[k];
  const float mean = wave_sum(s) * (1.f / DM);
  float q = 0.f;
#pragma unroll
  for (int k = 0; k < 8; ++k) { const float d = v[k] - mean; q = fmaf(d, d, q); }
  const float var = wave_sum(q) * (1.f / DM);
  const float mb = rbf(mean);
  const float den = rbf(sqrtf(rbf(rbf(var) + 1e-5f)));
#pragma unroll
  for (int k = 0; k < 4; ++k) o[k] = pk2(rbf(v[2 * k] - mb) / den, rbf(v[2 * k + 1] - mb) / den);
}

template <int I>
__device__ __forceinline__ u32x4 ldkv(__amdgpu_buffer_rsrc_t rs, int voff, int soff) {
  return __builtin_amdgcn_raw_buffer_load_b128(rs, voff + (I & 3) * 1024, soff + (I >> 2) * 4096, KV_NT ? 2 : 0);
}
__device__ __forceinline__ int opaque(int v) { asm volatile("" : "+v"(v)); return v; }
#define SB() __builtin_amdgcn_sched_barrier(0)

__device__ __forceinline__ void st_sc1_b64(void* p, float a, float b) {
  unsigned ua = bc<unsigned>(a), ub = bc<unsigned>(b);
  ua = (ua == SENT) ? 0x7FC00000u : ua; ub = (ub == SENT) ? 0x7FC00000u : ub;
  __hip_atomic_store(reinterpret_cast<unsigned long long*>(p), ((unsigned long long)ub << 32) | ua, __ATOMIC_RELAXED, __HIP_MEMORY_SCOPE_AGENT);
}

__device__ __forceinline__ int chunk0(int j) { return 5 * j + (j < 12 ? j : 12); }

__device__ __forceinline__ void pin(u32x4& v) { asm volatile("" : "+v"(v)); }
__device__ __forceinline__ void prep2(u32x4& w0, u32x4& w1) {
#pragma unroll
  for (int k = 0; k < 4; ++k) {
    const unsigned lo = __builtin_amdgcn_perm(w1[k], w0[k], 0x05040100u), hi = __builtin_amdgcn_perm(w1[k], w0[k], 0x07060302u);
    w0[k] = lo; w1[k] = hi;
  }
  pin(w0); pin(w1);
}
__device__ __forceinline__ void mv2p(const u32x4& w0, const u32x4& w1, unsigned xp, float (&acc)[8]) {
#pragma unroll
  for (int k = 0; k < 4; ++k) { acc[2 * k] = dot2(w0[k], xp, acc[2 * k]); acc[2 * k + 1] = dot2(w1[k], xp, acc[2 * k + 1]); }
}
__device__ __forceinline__ unsigned ld32(const void* p) { return __hip_atomic_load(reinterpret_cast<const unsigned*>(p), __ATOMIC_RELAXED, __HIP_MEMORY_SCOPE_AGENT); }
__device__ __forceinline__ unsigned long long ld64(const void* p) { return __hip_atomic_load(reinterpret_cast<const unsigned long long*>(p), __ATOMIC_RELAXED, __HIP_MEMORY_SCOPE_AGENT); }
__device__ __forceinline__ void st32(void* p, unsigned v) { __hip_atomic_store(reinterpret_cast<unsigned*>(p), v, __ATOMIC_RELAXED, __HIP_MEMORY_SCOPE_AGENT); }
__device__ __forceinline__ void st64(void* p, unsigned long long v) { __hip_atomic_store(reinterpret_cast<unsigned long long*>(p), v, __ATOMIC_RELAXED, __HIP_MEMORY_SCOPE_AGENT); }
__device__ __forceinline__ unsigned rfl(unsigned v) { return (unsigned)__builtin_amdgcn_readfirstlane((int)v); }

__device__ __forceinline__ bool nosent(const u32x4& v) { return v[0] != SENT && v[1] != SENT && v[2] != SENT && v[3] != SENT; }
__device__ __forceinline__ int rot_of(int b, int h) { return 16 * (h >> 2) + 2 * b; }

__global__ void __launch_bounds__(NT, 2)
fused_kernel(const u16* __restrict__ x, const u16* __restrict__ kc, const u16* __restrict__ vc, const u16* __restrict__ wq,
             const u16* __restrict__ wo, const u16* __restrict__ wg, const u16* __restrict__ wu, const u16* __restrict__ wd,
             u16* __restrict__ out, unsigned char* ws) {
  __shared__ __attribute__((aligned(16))) unsigned char lds[L_END];
  const int tid = threadIdx.x, lane = tid & 63;
  const int w = __builtin_amdgcn_readfirstlane(tid >> 6);
  const int r = lane >> 4, c = lane & 15;
  const int bid = blockIdx.x;
  const int h = (bid & 7) * 4 + ((bid >> 3) & 3), b = bid >> 5;
  int rot8 = rot_of(b, h) + w;
  const int nch = h < 12 ? 6 : 5;

  const __amdgpu_buffer_rsrc_t rK = __builtin_amdgcn_make_buffer_rsrc((void*)(kc + (size_t)(b * NH + h) * SK * DH), 0, SK * DH * 2, 0x00020000);
  const __amdgpu_buffer_rsrc_t rV = __builtin_amdgcn_make_buffer_rsrc((void*)(vc + (size_t)(b * NH + h) * SK * DH), 0, SK * DH * 2, 0x00020000);
  const __amdgpu_buffer_rsrc_t rs = __builtin_amdgcn_make_buffer_rsrc((void*)(ws + WS_SLOT + (size_t)b * NH * SLOT), 0, NH * SLOT, 0x00020000);
  unsigned char* const slotb = ws + WS_SLOT + (size_t)b * NH * SLOT;
  const int voff = lane * 16;
#define GOFF(g) (((((g) & 15) * 8 + rot8) & 127) * 8192)

  TSDECL
  STAMP(0);
  unsigned char* const hsb = ws + WS_HS + (size_t)b * 1024;
  const bool lead = h == 0;
  if (tid < 256) __builtin_amdgcn_raw_buffer_store_b128(u32x4{SENT, SENT, SENT, SENT}, rs, tid * 16, h * SLOT, 16);
  unsigned rv = ld32(hsb + 512 + h * 4), gv = 0u;
  unsigned long long ab = ld64(hsb + 256 + (lane & 31) * 8);
  SB();
  const u32x4 xr = *reinterpret_cast<const u32x4*>(x + b * DM + lane * 8);
  u32x4 A[16], X[16], R[8], DW[6];
  {
    const u16* base = wq + (size_t)(64 * w + 16 * r) * (NH * DH) + h * DH + 8 * c;
#define IL(e) A[e] = WLD(base + (e) * (NH * DH)); SB(); R[e] = ldkv<e>(rK, voff, GOFF(0)); SB();
    IL(0) IL(1) IL(2) IL(3) IL(4) IL(5) IL(6) IL(7)
#undef IL
#define IL(e) A[8 + e] = WLD(base + (8 + e) * (NH * DH)); SB(); X[e] = ldkv<e>(rK, voff, GOFF(1)); SB();
    IL(0) IL(1) IL(2) IL(3) IL(4) IL(5) IL(6) IL(7)
#undef IL
  }
  SB();
#define LD8(dst, o, rs_, so) dst[o + 0] = ldkv<0>(rs_, voff, so); dst[o + 1] = ldkv<1>(rs_, voff, so); dst[o + 2] = ldkv<2>(rs_, voff, so); dst[o + 3] = ldkv<3>(rs_, voff, so); \
                             dst[o + 4] = ldkv<4>(rs_, voff, so); dst[o + 5] = ldkv<5>(rs_, voff, so); dst[o + 6] = ldkv<6>(rs_, voff, so); dst[o + 7] = ldkv<7>(rs_, voff, so);
  LD8(X, 8, rK, GOFF(2))
  SB();
  const unsigned r1 = rfl(rv) + 1u;
  unsigned tagN = 0u, eg = 0u;
  bool ech = false, granted = false, ready = false;
  if (lead && w == 0) {
    while (__any((unsigned)ab == tagN)) ++tagN;
    if (lane == 0) st32(hsb, tagN);
  }
#define HS_INIT if (w == 0) { gv = ld32(hsb); rv = ld32(hsb + 512 + h * 4); }
#define HS_STEP if (w == 0 && !ready) { \
    const unsigned g_ = rfl(gv), r_ = rfl(rv); \
    if (r_ == r1) ready = true; \
    else { \
      if (!ech || g_ != eg) { if (lane == 0) st64(hsb + 256 + h * 8, (unsigned long long)g_ | ((unsigned long long)r1 << 32)); eg = g_; ech = true; } \
      if (lead && !granted) { if (__all((unsigned)ab == tagN)) { if (lane < 32) st32(hsb + 512 + lane * 4, (unsigned)(ab >> 32)); granted = true; } } \
      gv = ld32(hsb); rv = ld32(hsb + 512 + h * 4); \
      if (lead) ab = ld64(hsb + 256 + (lane & 31) * 8); \
    } }
#define ROWRED(av, z0, z1) float z0, z1; { float y_[4]; \
    _Pragma("unroll") for (int k = 0; k < 4; ++k) y_[k] = swapadd32(av[k], av[k + 4]); \
    z0 = swapadd16(y_[0], y_[2]); z1 = swapadd16(y_[1], y_[3]); }

  {
    float xv[8];
#pragma unroll
    for (int k = 0; k < 4; ++k) { xv[2 * k] = bflo(xr[k]); xv[2 * k + 1] = bfhi(xr[k]); }
    unsigned xn[4];
    ln_row(xv, xn);
    float acc[8] = {0.f, 0.f, 0.f, 0.f, 0.f, 0.f, 0.f, 0.f};
#pragma unroll
    for (int e = 0; e < 8; ++e) {
      const unsigned xp = (unsigned)__shfl((int)xn[e & 3], 8 * w + 2 * r + (e >> 2));
      mv2(A[2 * e], A[2 * e + 1], xp, acc);
    }
    ROWRED(acc, z0, z1)
    *reinterpret_cast<f32x2*>(lds + L_QP + (w * DH + 8 * c + 4 * (lane >> 5) + 2 * (r & 1)) * 4) = f32x2{z0, z1};
  }
  __syncthreads();
  if (tid < 64) {
    const f32x2* qp = reinterpret_cast<const f32x2*>(lds + L_QP);
    float a0 = 0.f, a1 = 0.f;
#pragma unroll
    for (int k = 0; k < NW; ++k) { const f32x2 t = qp[k * 64 + tid]; a0 += t[0]; a1 += t[1]; }
    reinterpret_cast<unsigned*>(lds + L_QF)[tid] = pk2(a0, a1);
  }
  __syncthreads();
  const u32x4 q = *reinterpret_cast<const u32x4*>(lds + L_QF + c * 16);

  STAMP(1);
  const u16* gub;
  {
    int cm = c < 2 * nch ? c : 2 * nch - 1;
    const bool up = cm >= nch;
    gub = (up ? wu : wg) + (size_t)(64 * w + r) * FF + (chunk0(h) + (up ? cm - nch : cm)) * 8;
  }
  float mx = -INFINITY;
  unsigned char* const psc = lds + L_P + (w * 64 + r) * 16 + (c >> 1) * 2;
#pragma unroll
  for (int g = 0; g < 16; ++g) {
    float xs[8];
    const int sc0 = GOFF(g), sn = GOFF(g + 1);
    const __amdgpu_buffer_rsrc_t rn = (g < 15) ? rK : rV;
    const int l1 = opaque(lane);
    const u16* wdb = wd + (size_t)(chunk0(h) * 8 + (l1 >> 3)) * DM + 64 * w + 8 * (l1 & 7);
    if (g == 3) { HS_INIT }
    if (g > 3 && (g & 1)) { HS_STEP }
    SB();
#define KDOT(src) { float d = dot2(src[0], q[0], 0.f); d = dot2(src[1], q[1], d); d = dot2(src[2], q[2], d); d = dot2(src[3], q[3], d); asm volatile("" : "+v"(d)); xs[i_] = d; }
#define KSTEP(i) { constexpr int i_ = i; \
      if (g == 1) { KDOT(X[i]) SB(); } \
      else if (g == 2) { KDOT(X[8 + i]) SB(); } \
      else { KDOT(R[i]) SB(); \
        if (g == 0) R[i] = ldkv<i>(rK, voff, GOFF(3)); \
        else if (i >= 4) R[(i + 4) & 7] = ldkv<(i + 4) & 7>(rn, voff, sn); \
        else if (g > 3) R[(i + 4) & 7] = ldkv<(i + 4) & 7>(rK, voff, sc0); \
      } \
      if (g >= 3 && g < 14 && ((i) & 3) == 1) { const int k_ = (g - 3) * 2 + ((i) >> 2); \
        if (k_ < 16) X[k_ & 15] = WLD(gub + (k_ & 15) * 4 * FF); \
        else { const int d_ = (k_ - 16) % 6; DW[d_] = WLD(wdb + (d_ < 5 || nch == 6 ? d_ : 0) * 8 * DM); } } \
      SB(); }
    KSTEP(0) KSTEP(1) KSTEP(2) KSTEP(3) KSTEP(4) KSTEP(5) KSTEP(6) KSTEP(7)
#undef KSTEP
#undef KDOT
#pragma unroll
    for (int k = 0; k < 4; ++k) {
      const float t1 = xs[k] + dpp<ROR8>(xs[k]), t2 = xs[k + 4] + dpp<ROR8>(xs[k + 4]);
      xs[k] = (c & 8) ? t2 : t1;
    }
#pragma unroll
    for (int k = 0; k < 2; ++k) {
      const float t1 = xs[k] + dpp<XOR7>(xs[k]), t2 = xs[k + 2] + dpp<XOR7>(xs[k + 2]);
      xs[k] = (c & 4) ? t2 : t1;
    }
    const float t1 = xs[0] + dpp<XOR2>(xs[0]), t2 = xs[1] + dpp<XOR2>(xs[1]);
    float s = (c & 2) ? t2 : t1;
    s += dpp<XOR1>(s);
    const __bf16 sb = (__bf16)(rbf(s) * 0.08838834764831845f);
    mx = fmaxf(mx, (float)sb);
    if ((c & 1) == 0) *reinterpret_cast<u16*>(psc + g * 64) = bc<u16>(sb);
  }

  STAMP(2);
  {
    u32x4* const pcell = reinterpret_cast<u32x4*>(lds + L_P + (w * 64 + lane) * 16);
    const u32x4 sv = *pcell;
    const float m = wave_max(mx);
    float l = 0.f;
#pragma unroll
    for (int k = 0; k < 4; ++k) l += expf(bflo(sv[k]) - m) + expf(bfhi(sv[k]) - m);
    l = wave_sum(l);
    if (lane == 0) *reinterpret_cast<f32x2*>(lds + L_ST + w * 8) = f32x2{m, l};
    __syncthreads();
    const f32x2 st = *reinterpret_cast<const f32x2*>(lds + L_ST + (c & 7) * 8);
    const float M = row_max(st[0]);
    const float L = row_sum(st[1] * expf(st[0] - M)) * 0.5f;
    u32x4 pv;
#pragma unroll
    for (int k = 0; k < 4; ++k) pv[k] = pk2(expf(bflo(sv[k]) - M) / L, expf(bfhi(sv[k]) - M) / L);
    *pcell = pv;
  }

  STAMP(3);
  float acc[8] = {0.f, 0.f, 0.f, 0.f, 0.f, 0.f, 0.f, 0.f};
  asm volatile("" : "+s"(rot8));
#define VMAC(ip) mv2(R[2 * ip], R[2 * ip + 1], pw[ip], acc); \
      asm volatile("" : "+v"(acc[0]), "+v"(acc[1]), "+v"(acc[2]), "+v"(acc[3]), "+v"(acc[4]), "+v"(acc[5]), "+v"(acc[6]), "+v"(acc[7])); SB();
#define VLD(i, so) R[i] = ldkv<i>(rV, voff, so); R[i + 1] = ldkv<i + 1>(rV, voff, so);
#pragma unroll
  for (int g = 0; g < 16; ++g) {
    const u32x4 pw = *reinterpret_cast<const u32x4*>(lds + L_P + ((w * 16 + g) * 4 + r) * 16);
    const int sc0 = GOFF(g), sn = GOFF(g + 1);
    const int l1 = opaque(lane);
    const u16* wob = wo + (size_t)(h * DH + (l1 >> 3)) * DM + 64 * w + 8 * (l1 & 7);
    if ((g & 3) == 1) { HS_STEP }
    if (g < 8) prep2(X[2 * (g & 7)], X[2 * (g & 7) + 1]); else prep2(A[2 * (g & 7)], A[2 * (g & 7) + 1]);
    if (g == 10 && nch == 5) DW[5] = u32x4{0u, 0u, 0u, 0u};
    if (g >= 8 && g < 11) prep2(DW[(2 * g - 16) % 6], DW[(2 * g - 15) % 6]);
    SB();
    if (g < DEEP) {
      VMAC(0) VLD(4, sc0) if (g < 8) A[2 * (g & 7)] = WLD(wob + 2 * (g & 7) * 8 * DM); SB();
      VMAC(1) VLD(6, sc0) SB();
      VMAC(2) if (g < 15) { VLD(0, sn) } if (g < 8) A[2 * (g & 7) + 1] = WLD(wob + (2 * (g & 7) + 1) * 8 * DM); SB();
      VMAC(3) if (g < 15) { VLD(2, sn) } SB();
      if (g == DEEP - 1 && g < 15) { VLD(4, sn) VLD(6, sn) SB(); }
    } else {
      VMAC(0) if (g < 15) { VLD(0, sn) } SB();
      VMAC(1) if (g < 15) { VLD(2, sn) } SB();
      VMAC(2) if (g < 15) { VLD(4, sn) } SB();
      VMAC(3) if (g < 15) { VLD(6, sn) } SB();
    }
  }
#undef VLD
#undef VMAC
  STAMP(4);
  const int ln2 = opaque(lane), r2 = ln2 >> 4, c2 = ln2 & 15;
  const int hcol = 64 * w + 4 * c2 + 2 * (ln2 >> 5) + (r2 & 1);
  const unsigned xcol = x[b * DM + hcol];
  {
    ROWRED(acc, z0, z1)
    *reinterpret_cast<f32x2*>(lds + L_QP + (w * DH + 8 * c2 + 4 * (ln2 >> 5) + 2 * (r2 & 1)) * 4) = f32x2{z0, z1};
  }
  for (unsigned spin = 0; w == 0 && !ready && spin < SPIN_CAP; ++spin) { HS_STEP __builtin_amdgcn_s_sleep(2); }
#undef HS_STEP
#undef HS_INIT
  __syncthreads();
  STAMP(10);
  {
    const int d = (ln2 >> 3) * 16 + (ln2 & 7);
    const float* op = reinterpret_cast<const float*>(lds + L_QP) + d;
    float s0 = 0.f, s1 = 0.f;
#pragma unroll
    for (int k = 0; k < NW; ++k) { s0 += op[k * DH]; s1 += op[k * DH + 8]; }
    *reinterpret_cast<unsigned*>(lds + L_OF + w * 256 + (ln2 & 7) * 32 + (ln2 >> 3) * 4) = pk2(s0, s1);
    __builtin_amdgcn_wave_barrier();
  }
  STAMP(11);
  unsigned char* const myslot = slotb + h * SLOT + (64 * w + 8 * (ln2 & 7) + 4 * (ln2 >> 5) + 2 * (r2 & 1)) * 4;
  {
    const u32x4 ov0 = *reinterpret_cast<const u32x4*>(lds + L_OF + w * 256 + (ln2 >> 3) * 32);
    const u32x4 ov1 = *reinterpret_cast<const u32x4*>(lds + L_OF + w * 256 + (ln2 >> 3) * 32 + 16);
    float pa[8] = {0.f, 0.f, 0.f, 0.f, 0.f, 0.f, 0.f, 0.f};
#pragma unroll
    for (int e = 0; e < 16; ++e) pin(A[e]);
#pragma unroll
    for (int e = 0; e < 4; ++e) mv2p(A[2 * e], A[2 * e + 1], ov0[e], pa);
#pragma unroll
    for (int e = 0; e < 4; ++e) mv2p(A[8 + 2 * e], A[8 + 2 * e + 1], ov1[e], pa);
    ROWRED(pa, z0, z1)
    z0 += dpp<ROR8>(z0); z1 += dpp<ROR8>(z1);
    if ((ln2 & 8) == 0) st_sc1_b64(myslot + S_P1, z0, z1);
  }
  STAMP(5);

#undef LD8
  STAMP(12);

  float hv;
  {
    const int off0 = r2 * SLOT + S_P1 + (64 * w + 4 * c2) * 4;
    u32x4 v[8];
    bool done[8] = {false, false, false, false, false, false, false, false};
    for (unsigned spin = 0;; ++spin) {
      const int o_ = opaque(off0);
#pragma unroll
      for (int i = 0; i < 8; ++i) if (!done[i]) v[i] = __builtin_amdgcn_raw_buffer_load_b128(rs, o_ + i * 4 * SLOT, 0, 16);
      bool all = true;
#pragma unroll
      for (int i = 0; i < 8; ++i) { if (!done[i]) done[i] = __all(nosent(v[i])); all = all && done[i]; }
      if (all || spin > SPIN_CAP) break;
      __builtin_amdgcn_s_sleep(1);
    }
    STAMP(6);
    float s[4];
#pragma unroll
    for (int k = 0; k < 4; ++k) {
      s[k] = bc<float>(v[0][k]);
#pragma unroll
      for (int i = 1; i < 8; ++i) s[k] += bc<float>(v[i][k]);
    }
    const float y0 = swapadd32(s[0], s[2]), y1 = swapadd32(s[1], s[3]);
    hv = rbf(bflo(xcol) + rbf(swapadd16(y0, y1)));
    if ((hcol >> 4) == h) reinterpret_cast<float*>(lds + L_HR)[hcol & 15] = hv;
    const float mw = wave_sum(hv) * (1.f / 64.f);
    const float m2 = wave_sum((hv - mw) * (hv - mw));
    if (ln2 == 0) *reinterpret_cast<f32x2*>(lds + L_ST + 64 + w * 8) = f32x2{mw, m2};
  }
  __syncthreads();
  STAMP(13);
  {
    const f32x2 st = *reinterpret_cast<const f32x2*>(lds + L_ST + 64 + (c2 & 7) * 8);
    const float mean = row_sum(st[0]) * (0.5f / 8.f);
    const float var = row_sum(fmaf(64.f * (st[0] - mean), st[0] - mean, st[1])) * (0.5f / DM);
    const float den = rbf(sqrtf(rbf(rbf(var) + 1e-5f)));
    const float hn = rbf(rbf(hv - rbf(mean)) / den);
    const unsigned hp = __builtin_amdgcn_perm(bc<unsigned>(dpp<XOR1>(hn)), bc<unsigned>(hn), 0x07060302u);
    float ga[8] = {0.f, 0.f, 0.f, 0.f, 0.f, 0.f, 0.f, 0.f};
#pragma unroll
    for (int e = 0; e < 16; ++e) pin(X[e]);
#define GU(e) mv2p(X[2 * e], X[2 * e + 1], (unsigned)__builtin_amdgcn_mov_dpp((int)hp, 0x150 + 2 * e, 0xf, 0xf, true), ga);
    GU(0) GU(1) GU(2) GU(3) GU(4) GU(5) GU(6) GU(7)
#undef GU
    ROWRED(ga, z0, z1)
    if (c2 < 2 * nch) *reinterpret_cast<f32x2*>(lds + L_GP + (w * 96 + c2 * 8 + 4 * (ln2 >> 5) + 2 * (r2 & 1)) * 4) = f32x2{z0, z1};
  }
  STAMP(14);
  __syncthreads();
  STAMP(15);
  {
    const float* gp = reinterpret_cast<const float*>(lds + L_GP);
    const bool on = ln2 < 8 * nch;
    const int li = on ? ln2 : 0;
    float g = 0.f, u = 0.f;
#pragma unroll
    for (int k = 0; k < NW; ++k) { g += gp[k * 96 + li]; u += gp[k * 96 + 8 * nch + li]; }
    g = rbf(g); u = rbf(u);
    const float sg = rbf(g / (1.f + expf(-g)));
    const float a = on ? rbf(sg * u) : 0.f;
    const unsigned ap = __builtin_amdgcn_perm(bc<unsigned>(dpp<ROR8>(a)), bc<unsigned>(a), 0x07060302u);
#pragma unroll
    for (int e = 0; e < 6; ++e) pin(DW[e]);
    float da[8] = {0.f, 0.f, 0.f, 0.f, 0.f, 0.f, 0.f, 0.f};
#pragma unroll
    for (int e = 0; e < 3; ++e) {
      const unsigned xp = (unsigned)__shfl((int)ap, 16 * e + (ln2 >> 3));
      mv2p(DW[2 * e], DW[2 * e + 1], xp, da);
    }
    ROWRED(da, z0, z1)
    z0 += dpp<ROR8>(z0); z1 += dpp<ROR8>(z1);
    if ((ln2 & 8) == 0) st_sc1_b64(myslot + S_P2, z0, z1);
  }
  STAMP(7);

  if (w == 0) {
    const int off0 = (ln2 >> 2) * SLOT + S_P2 + (16 * h + 4 * (ln2 & 3)) * 4;
    u32x4 v0, v1;
    for (unsigned spin = 0;; ++spin) {
      const int o_ = opaque(off0);
      v0 = __builtin_amdgcn_raw_buffer_load_b128(rs, o_, 0, 16);
      v1 = __builtin_amdgcn_raw_buffer_load_b128(rs, o_ + 16 * SLOT, 0, 16);
      if (__all(nosent(v0) && nosent(v1)) || spin > SPIN_CAP) break;
      __builtin_amdgcn_s_sleep(1);
    }
    STAMP(8);
    float s[4];
#pragma unroll
    for (int k = 0; k < 4; ++k) s[k] = bc<float>(v0[k]) + bc<float>(v1[k]);
    const float y0 = swapadd32(s[0], s[2]), y1 = swapadd32(s[1], s[3]);
    float z = swapadd16(y0, y1);
    z += dpp<ROR8>(z); z += dpp<ROR4>(z);
    if ((ln2 & 12) == 0) {
      const int col = 4 * (ln2 & 3) + 2 * (ln2 >> 5) + (r2 & 1);
      const float hres = reinterpret_cast<const float*>(lds + L_HR)[col];
      const __bf16 o = (__bf16)(hres + rbf(z));
      out[b * DM + 16 * h + col] = bc<u16>(o);
    }
  }
  STAMP(9);
  TSFLUSH;
#undef GOFF
#undef ROWRED
}

}

extern "C" void kernel_launch(void* const* d_in, const int* in_sizes, int n_in,
                              void* d_out, int out_size, void* d_ws, size_t ws_size,
                              hipStream_t stream) {
  const u16* x = (const u16*)d_in[0];
  const u16* kc = (const u16*)d_in[1];
  const u16* vc = (const u16*)d_in[2];
  const u16* wq = (const u16*)d_in[3];
  const u16* wo = (const u16*)d_in[4];
  const u16* wg = (const u16*)d_in[6];
  const u16* wu = (const u16*)d_in[7];
  const u16* wd = (const u16*)d_in[8];
  u16* out = (u16*)d_out;
  unsigned char* ws = (unsigned char*)d_ws;
  hipLaunchKernelGGL(fused_kernel, dim3(NB * NH), dim3(NT), 0, stream, x, kc, vc, wq, wo, wg, wu, wd, out, ws);
}
```

```cpp
#include <hip/hip_runtime.h>
#include <hip/hip_bf16.h>
#include <stdint.h>

#define WLD(p) __builtin_nontemporal_load(reinterpret_cast<const u32x4*>(p))
#define KV_NT 1

#define STAMP(k)
#define TSARG
#define TSPASS
#define TSDECL
#define TSFLUSH

namespace {

constexpr int NB = 8, DM = 512, NH = 32, SK = 4096, DH = 128, FF = 1376;
constexpr int NW = 8, NT = NW * 64;
constexpr unsigned SENT = 0xFFFFFFFFu;
constexpr unsigned SPIN_CAP = 400000u;
constexpr int PSL = 1;

using u16 = unsigned short;
using u32x4 = __attribute__((ext_vector_type(4))) unsigned;
using u32x2 = __attribute__((ext_vector_type(2))) unsigned;
using f32x4 = __attribute__((ext_vector_type(4))) float;
using f32x2 = __attribute__((ext_vector_type(2))) float;
using bf16x2 = __attribute__((ext_vector_type(2))) __bf16;

constexpr int S_P1 = 0;
constexpr int S_P2 = 2048;
constexpr int SLOT = 4096;
constexpr size_t WS_SLOT = 0;
constexpr size_t SLOT_BYTES = (size_t)NB * NH * SLOT;
constexpr size_t WS_HS = WS_SLOT + SLOT_BYTES;
constexpr int DEEP = 12;

constexpr int L_P = 0;
constexpr int L_QP = L_P + 8192;
constexpr int L_QF = L_QP + 4096;
constexpr int L_ST = L_QF + 256;
constexpr int L_OF = L_ST + 128;
constexpr int L_HR = L_OF + 2048;
constexpr int L_GP = L_HR + 64;
constexpr int L_GS = L_GP + 8 * 96 * 4;
constexpr int L_END = L_GS + 8 * 96 * 4;

template <typename T, typename F> __device__ __forceinline__ T bc(F x) { return __builtin_bit_cast(T, x); }
__device__ __forceinline__ float rbf(float x) { return (float)(__bf16)x; }
__device__ __forceinline__ unsigned pk2(float lo, float hi) { bf16x2 v = {(__bf16)lo, (__bf16)hi}; return bc<unsigned>(v); }
__device__ __forceinline__ float bflo(unsigned u) { return bc<float>(u << 16); }
__device__ __forceinline__ float bfhi(unsigned u) { return bc<float>(u & 0xffff0000u); }
__device__ __forceinline__ float dot2(unsigned a, unsigned b, float c) {
  return __builtin_amdgcn_fdot2_f32_bf16(bc<bf16x2>(a), bc<bf16x2>(b), c, false);
}
template <int CTRL> __device__ __forceinline__ float dpp(float x) {
  return bc<float>(__builtin_amdgcn_mov_dpp(bc<int>(x), CTRL, 0xf, 0xf, true));
}
constexpr int XOR1 = 0xB1, XOR2 = 0x4E, XOR7 = 0x141, ROR8 = 0x128, ROR4 = 0x124;
__device__ __forceinline__ float swapadd32(float a, float b) {
  auto s = __builtin_amdgcn_permlane32_swap(bc<unsigned>(a), bc<unsigned>(b), false, false);
  return bc<float>(s[0]) + bc<float>(s[1]);
}
__device__ __forceinline__ float swapadd16(float a, float b) {
  auto s = __builtin_amdgcn_permlane16_swap(bc<unsigned>(a), bc<unsigned>(b), false, false);
  return bc<float>(s[0]) + bc<float>(s[1]);
}
__device__ __forceinline__ float swapmax32(float a) {
  auto s = __builtin_amdgcn_permlane32_swap(bc<unsigned>(a), bc<unsigned>(a), false, false);
  return fmaxf(bc<float>(s[0]), bc<float>(s[1]));
}
__device__ __forceinline__ float swapmax16(float a) {
  auto s = __builtin_amdgcn_permlane16_swap(bc<unsigned>(a), bc<unsigned>(a), false, false);
  return fmaxf(bc<float>(s[0]), bc<float>(s[1]));
}
__device__ __forceinline__ float row_sum(float x) {
  x += dpp<XOR1>(x); x += dpp<XOR2>(x); x += dpp<XOR7>(x); x += dpp<ROR8>(x); return x;
}
__device__ __forceinline__ float row_max(float x) {
  x = fmaxf(x, dpp<XOR1>(x)); x = fmaxf(x, dpp<XOR2>(x)); x = fmaxf(x, dpp<XOR7>(x)); x = fmaxf(x, dpp<ROR8>(x)); return x;
}
__device__ __forceinline__ float wave_sum(float x) { x = row_sum(x); x = swapadd16(x, x); return swapadd32(x, x); }
__device__ __forceinline__ float wave_max(float x) { x = row_max(x); x = swapmax16(x); return swapmax32(x); }

__device__ __forceinline__ void mv2(const u32x4& w0, const u32x4& w1, unsigned xp, float (&acc)[8]) {
#pragma unroll
  for (int k = 0; k < 4; ++k) {
    const unsigned lo = __builtin_amdgcn_perm(w1[k], w0[k], 0x05040100u);
    const unsigned hi = __builtin_amdgcn_perm(w1[k], w0[k], 0x07060302u);
    acc[2 * k] = dot2(lo, xp, acc[2 * k]);
    acc[2 * k + 1] = dot2(hi, xp, acc[2 * k + 1]);
  }
}

__device__ __forceinline__ void ln_row(const float (&v)[8], unsigned (&o)[4]) {
  float s = 0.f;
#pragma unroll
  for (int k = 0; k < 8; ++k) s += v[k];
  const float mean = wave_sum(s) * (1.f / DM);
  float q = 0.f;
#pragma unroll
  for (int k = 0; k < 8; ++k) { const float d = v[k] - mean; q = fmaf(d, d, q); }
  const float var = wave_sum(q) * (1.f / DM);
  const float mb = rbf(mean);
  const float den = rbf(sqrtf(rbf(rbf(var) + 1e-5f)));
#pragma unroll
  for (int k = 0; k < 4; ++k) o[k] = pk2(rbf(v[2 * k] - mb) / den, rbf(v[2 * k + 1] - mb) / den);
}

template <int I>
__device__ __forceinline__ u32x4 ldkv(__amdgpu_buffer_rsrc_t rs, int voff, int soff) {
  return __builtin_amdgcn_raw_buffer_load_b128(rs, voff + (I & 3) * 1024, soff + (I >> 2) * 4096, KV_NT ? 2 : 0);
}
__device__ __forceinline__ int opaque(int v) { asm volatile("" : "+v"(v)); return v; }
#define SB() __builtin_amdgcn_sched_barrier(0)

__device__ __forceinline__ void st_sc1_b64(void* p, float a, float b) {
  unsigned ua = bc<unsigned>(a), ub = bc<unsigned>(b);
  ua = (ua == SENT) ? 0x7FC00000u : ua; ub = (ub == SENT) ? 0x7FC00000u : ub;
  __hip_atomic_store(reinterpret_cast<unsigned long long*>(p), ((unsigned long long)ub << 32) | ua, __ATOMIC_RELAXED, __HIP_MEMORY_SCOPE_AGENT);
}

__device__ __forceinline__ int chunk0(int j) { return 5 * j + (j < 12 ? j : 12); }

__device__ __forceinline__ void pin(u32x4& v) { asm volatile("" : "+v"(v)); }
__device__ __forceinline__ void prep2(u32x4& w0, u32x4& w1) {
#pragma unroll
  for (int k = 0; k < 4; ++k) {
    const unsigned lo = __builtin_amdgcn_perm(w1[k], w0[k], 0x05040100u), hi = __builtin_amdgcn_perm(w1[k], w0[k], 0x07060302u);
    w0[k] = lo; w1[k] = hi;
  }
  pin(w0); pin(w1);
}
__device__ __forceinline__ void mv2p(const u32x4& w0, const u32x4& w1, unsigned xp, float (&acc)[8]) {
#pragma unroll
  for (int k = 0; k < 4; ++k) { acc[2 * k] = dot2(w0[k], xp, acc[2 * k]); acc[2 * k + 1] = dot2(w1[k], xp, acc[2 * k + 1]); }
}
__device__ __forceinline__ unsigned ld32(const void* p) { return __hip_atomic_load(reinterpret_cast<const unsigned*>(p), __ATOMIC_RELAXED, __HIP_MEMORY_SCOPE_AGENT); }
__device__ __forceinline__ unsigned long long ld64(const void* p) { return __hip_atomic_load(reinterpret_cast<const unsigned long long*>(p), __ATOMIC_RELAXED, __HIP_MEMORY_SCOPE_AGENT); }
__device__ __forceinline__ void st32(void* p, unsigned v) { __hip_atomic_store(reinterpret_cast<unsigned*>(p), v, __ATOMIC_RELAXED, __HIP_MEMORY_SCOPE_AGENT); }
__device__ __forceinline__ void st64(void* p, unsigned long long v) { __hip_atomic_store(reinterpret_cast<unsigned long long*>(p), v, __ATOMIC_RELAXED, __HIP_MEMORY_SCOPE_AGENT); }
__device__ __forceinline__ unsigned rfl(unsigned v) { return (unsigned)__builtin_amdgcn_readfirstlane((int)v); }

__device__ __forceinline__ bool nosent(const u32x4& v) { return v[0] != SENT && v[1] != SENT && v[2] != SENT && v[3] != SENT; }
__device__ __forceinline__ int rot_of(int b, int h) { return 16 * (h >> 2) + 2 * b; }

__global__ void __launch_bounds__(NT, 2)
fused_kernel(const u16* __restrict__ x, const u16* __restrict__ kc, const u16* __restrict__ vc, const u16* __restrict__ wq,
             const u16* __restrict__ wo, const u16* __restrict__ wg, const u16* __restrict__ wu, const u16* __restrict__ wd,
             u16* __restrict__ out, unsigned char* ws) {
  __shared__ __attribute__((aligned(16))) unsigned char lds[L_END];
  const int tid = threadIdx.x, lane = tid & 63;
  const int w = __builtin_amdgcn_readfirstlane(tid >> 6);
  const int r = lane >> 4, c = lane & 15;
  const int bid = blockIdx.x;
  const int h = (bid & 7) * 4 + ((bid >> 3) & 3), b = bid >> 5;
  int rot8 = rot_of(b, h) + w;
  const int nch = h < 12 ? 6 : 5;

  const __amdgpu_buffer_rsrc_t rK = __builtin_amdgcn_make_buffer_rsrc((void*)(kc + (size_t)(b * NH + h) * SK * DH), 0, SK * DH * 2, 0x00020000);
  const __amdgpu_buffer_rsrc_t rV = __builtin_amdgcn_make_buffer_rsrc((void*)(vc + (size_t)(b * NH + h) * SK * DH), 0, SK * DH * 2, 0x00020000);
  const __amdgpu_buffer_rsrc_t rs = __builtin_amdgcn_make_buffer_rsrc((void*)(ws + WS_SLOT + (size_t)b * NH * SLOT), 0, NH * SLOT, 0x00020000);
  unsigned char* const slotb = ws + WS_SLOT + (size_t)b * NH * SLOT;
  const int voff = lane * 16;
#define GOFF(g) (((((g) & 15) * 8 + rot8) & 127) * 8192)

  TSDECL
  STAMP(0);
  unsigned char* const hsb = ws + WS_HS + (size_t)b * 1024;
  const bool lead = h == 0;
  if (tid < 256) __builtin_amdgcn_raw_buffer_store_b128(u32x4{SENT, SENT, SENT, SENT}, rs, tid * 16, h * SLOT, 16);
  unsigned rv = ld32(hsb + 512 + h * 4), gv = 0u;
  unsigned long long ab = ld64(hsb + 256 + (lane & 31) * 8);
  SB();
  const u32x4 xr = *reinterpret_cast<const u32x4*>(x + b * DM + lane * 8);
  u32x4 A[16], X[16], R[8], DW[6];
  {
    const u16* base = wq + (size_t)(64 * w + 16 * r) * (NH * DH) + h * DH + 8 * c;
#define IL(e) A[e] = WLD(base + (e) * (NH * DH)); SB(); R[e] = ldkv<e>(rK, voff, GOFF(0)); SB();
    IL(0) IL(1) IL(2) IL(3) IL(4) IL(5) IL(6) IL(7)
#undef IL
#define IL(e) A[8 + e] = WLD(base + (8 + e) * (NH * DH)); SB(); X[e] = ldkv<e>(rK, voff, GOFF(1)); SB();
    IL(0) IL(1) IL(2) IL(3) IL(4) IL(5) IL(6) IL(7)
#undef IL
  }
  SB();
#define LD8(dst, o, rs_, so) dst[o + 0] = ldkv<0>(rs_, voff, so); dst[o + 1] = ldkv<1>(rs_, voff, so); dst[o + 2] = ldkv<2>(rs_, voff, so); dst[o + 3] = ldkv<3>(rs_, voff, so); \
                             dst[o + 4] = ldkv<4>(rs_, voff, so); dst[o + 5] = ldkv<5>(rs_, voff, so); dst[o + 6] = ldkv<6>(rs_, voff, so); dst[o + 7] = ldkv<7>(rs_, voff, so);
  LD8(X, 8, rK, GOFF(2))
  SB();
  const unsigned r1 = rfl(rv) + 1u;
  unsigned tagN = 0u, eg = 0u;
  bool ech = false, granted = false, ready = false;
  if (lead && w == 0) {
    while (__any((unsigned)ab == tagN)) ++tagN;
    if (lane == 0) st32(hsb, tagN);
  }
#define HS_INIT if (w == 0) { gv = ld32(hsb); rv = ld32(hsb + 512 + h * 4); }
#define HS_STEP if (w == 0 && !ready) { \
    const unsigned g_ = rfl(gv), r_ = rfl(rv); \
    if (r_ == r1) ready = true; \
    else { \
      if (!ech || g_ != eg) { if (lane == 0) st64(hsb + 256 + h * 8, (unsigned long long)g_ | ((unsigned long long)r1 << 32)); eg = g_; ech = true; } \
      if (lead && !granted) { if (__all((unsigned)ab == tagN)) { if (lane < 32) st32(hsb + 512 + lane * 4, (unsigned)(ab >> 32)); granted = true; } } \
      gv = ld32(hsb); rv = ld32(hsb + 512 + h * 4); \
      if (lead) ab = ld64(hsb + 256 + (lane & 31) * 8); \
    } }
#define ROWRED(av, z0, z1) float z0, z1; { float y_[4]; \
    _Pragma("unroll") for (int k = 0; k < 4; ++k) y_[k] = swapadd32(av[k], av[k + 4]); \
    z0 = swapadd16(y_[0], y_[2]); z1 = swapadd16(y_[1], y_[3]); }

  {
    float xv[8];
#pragma unroll
    for (int k = 0; k < 4; ++k) { xv[2 * k] = bflo(xr[k]); xv[2 * k + 1] = bfhi(xr[k]); }
    unsigned xn[4];
    ln_row(xv, xn);
    float acc[8] = {0.f, 0.f, 0.f, 0.f, 0.f, 0.f, 0.f, 0.f};
#pragma unroll
    for (int e = 0; e < 8; ++e) {
      const unsigned xp = (unsigned)__shfl((int)xn[e & 3], 8 * w + 2 * r + (e >> 2));
      mv2(A[2 * e], A[2 * e + 1], xp, acc);
    }
    ROWRED(acc, z0, z1)
    *reinterpret_cast<f32x2*>(lds + L_QP + (w * DH + 8 * c + 4 * (lane >> 5) + 2 * (r & 1)) * 4) = f32x2{z0, z1};
  }
  __syncthreads();
  if (tid < 64) {
    const f32x2* qp = reinterpret_cast<const f32x2*>(lds + L_QP);
    float a0 = 0.f, a1 = 0.f;
#pragma unroll
    for (int k = 0; k < NW; ++k) { const f32x2 t = qp[k * 64 + tid]; a0 += t[0]; a1 += t[1]; }
    reinterpret_cast<unsigned*>(lds + L_QF)[tid] = pk2(a0, a1);
  }
  __syncthreads();
  const u32x4 q = *reinterpret_cast<const u32x4*>(lds + L_QF + c * 16);

  STAMP(1);
  const u16* gub;
  {
    int cm = c < 2 * nch ? c : 2 * nch - 1;
    const bool up = cm >= nch;
    gub = (up ? wu : wg) + (size_t)(64 * w + r) * FF + (chunk0(h) + (up ? cm - nch : cm)) * 8;
  }
  float mx = -INFINITY;
  unsigned char* const psc = lds + L_P + (w * 64 + r) * 16 + (c >> 1) * 2;
#pragma unroll
  for (int g = 0; g < 16; ++g) {
    float xs[8];
    const int sc0 = GOFF(g), sn = GOFF(g + 1);
    const __amdgpu_buffer_rsrc_t rn = (g < 15) ? rK : rV;
    const int l1 = opaque(lane);
    const u16* wdb = wd + (size_t)(chunk0(h) * 8 + (l1 >> 3)) * DM + 64 * w + 8 * (l1 & 7);
    if (g == 3) { HS_INIT }
    if (g > 3 && (g & 1)) { HS_STEP }
    SB();
#define KDOT(src) { float d = dot2(src[0], q[0], 0.f); d = dot2(src[1], q[1], d); d = dot2(src[2], q[2], d); d = dot2(src[3], q[3], d); asm volatile("" : "+v"(d)); xs[i_] = d; }
#define KSTEP(i) { constexpr int i_ = i; \
      if (g == 1) { KDOT(X[i]) SB(); } \
      else if (g == 2) { KDOT(X[8 + i]) SB(); } \
      else { KDOT(R[i]) SB(); \
        if (g == 0) R[i] = ldkv<i>(rK, voff, GOFF(3)); \
        else if (i >= 4) R[(i + 4) & 7] = ldkv<(i + 4) & 7>(rn, voff, sn); \
        else if (g > 3) R[(i + 4) & 7] = ldkv<(i + 4) & 7>(rK, voff, sc0); \
      } \
      if (g >= 3 && g < 14 && ((i) & 3) == 1) { const int k_ = (g - 3) * 2 + ((i) >> 2); \
        if (k_ < 16) X[k_ & 15] = WLD(gub + (k_ & 15) * 4 * FF); \
        else { const int d_ = (k_ - 16) % 6; DW[d_] = WLD(wdb + (d_ < 5 || nch == 6 ? d_ : 0) * 8 * DM); } } \
      SB(); }
    KSTEP(0) KSTEP(1) KSTEP(2) KSTEP(3) KSTEP(4) KSTEP(5) KSTEP(6) KSTEP(7)
#undef KSTEP
#undef KDOT
#pragma unroll
    for (int k = 0; k < 4; ++k) {
      const float t1 = xs[k] + dpp<ROR8>(xs[k]), t2 = xs[k + 4] + dpp<ROR8>(xs[k + 4]);
      xs[k] = (c & 8) ? t2 : t1;
    }
#pragma unroll
    for (int k = 0; k < 2; ++k) {
      const float t1 = xs[k] + dpp<XOR7>(xs[k]), t2 = xs[k + 2] + dpp<XOR7>(xs[k + 2]);
      xs[k] = (c & 4) ? t2 : t1;
    }
    const float t1 = xs[0] + dpp<XOR2>(xs[0]), t2 = xs[1] + dpp<XOR2>(xs[1]);
    float s = (c & 2) ? t2 : t1;
    s += dpp<XOR1>(s);
    const __bf16 sb = (__bf16)(rbf(s) * 0.08838834764831845f);
    mx = fmaxf(mx, (float)sb);
    if ((c & 1) == 0) *reinterpret_cast<u16*>(psc + g * 64) = bc<u16>(sb);
  }

  STAMP(2);
  {
    u32x4* const pcell = reinterpret_cast<u32x4*>(lds + L_P + (w * 64 + lane) * 16);
    const u32x4 sv = *pcell;
    const float m = wave_max(mx);
    float l = 0.f;
#pragma unroll
    for (int k = 0; k < 4; ++k) l += expf(bflo(sv[k]) - m) + expf(bfhi(sv[k]) - m);
    l = wave_sum(l);
    if (lane == 0) *reinterpret_cast<f32x2*>(lds + L_ST + w * 8) = f32x2{m, l};
    __syncthreads();
    const f32x2 st = *reinterpret_cast<const f32x2*>(lds + L_ST + (c & 7) * 8);
    const float M = row_max(st[0]);
    const float L = row_sum(st[1] * expf(st[0] - M)) * 0.5f;
    u32x4 pv;
#pragma unroll
    for (int k = 0; k < 4; ++k) pv[k] = pk2(expf(bflo(sv[k]) - M) / L, expf(bfhi(sv[k]) - M) / L);
    *pcell = pv;
  }

  STAMP(3);
  float acc[8] = {0.f, 0.f, 0.f, 0.f, 0.f, 0.f, 0.f, 0.f};
  float sa[8] = {0.f, 0.f, 0.f, 0.f, 0.f, 0.f, 0.f, 0.f};
  asm volatile("" : "+s"(rot8));
#define VMAC(ip) mv2(R[2 * ip], R[2 * ip + 1], pw[ip], acc); \
      asm volatile("" : "+v"(acc[0]), "+v"(acc[1]), "+v"(acc[2]), "+v"(acc[3]), "+v"(acc[4]), "+v"(acc[5]), "+v"(acc[6]), "+v"(acc[7])); SB();
#define VLD(i, so) R[i] = ldkv<i>(rV, voff, so); R[i + 1] = ldkv<i + 1>(rV, voff, so);
#pragma unroll
  for (int g = 0; g < 16; ++g) {
    const u32x4 pw = *reinterpret_cast<const u32x4*>(lds + L_P + ((w * 16 + g) * 4 + r) * 16);
    const int sc0 = GOFF(g), sn = GOFF(g + 1);
    const int l1 = opaque(lane);
    const u16* wob = wo + (size_t)(h * DH + (l1 >> 3)) * DM + 64 * w + 8 * (l1 & 7);
    if ((g & 3) == 1) { HS_STEP }
    if (g < 8) { prep2(X[2 * (g & 7)], X[2 * (g & 7) + 1]); mv2p(X[2 * (g & 7)], X[2 * (g & 7) + 1], 0x3F803F80u, sa); }
    else prep2(A[2 * (g & 7)], A[2 * (g & 7) + 1]);
    if (g == 8) {
      ROWRED(sa, z0, z1)
      if ((l1 & 15) < 2 * nch) *reinterpret_cast<f32x2*>(lds + L_GS + (w * 96 + (l1 & 15) * 8 + 4 * (l1 >> 5) + 2 * ((l1 >> 4) & 1)) * 4) = f32x2{z0, z1};
    }
    if (g == 10 && nch == 5) DW[5] = u32x4{0u, 0u, 0u, 0u};
    if (g >= 8 && g < 11) prep2(DW[(2 * g - 16) % 6], DW[(2 * g - 15) % 6]);
    SB();
    if (g < DEEP) {
      VMAC(0) VLD(4, sc0) if (g < 8) A[2 * (g & 7)] = WLD(wob + 2 * (g & 7) * 8 * DM); SB();
      VMAC(1) VLD(6, sc0) SB();
      VMAC(2) if (g < 15) { VLD(0, sn) } if (g < 8) A[2 * (g & 7) + 1] = WLD(wob + (2 * (g & 7) + 1) * 8 * DM); SB();
      VMAC(3) if (g < 15) { VLD(2, sn) } SB();
      if (g == DEEP - 1 && g < 15) { VLD(4, sn) VLD(6, sn) SB(); }
    } else {
      VMAC(0) if (g < 15) { VLD(0, sn) } SB();
      VMAC(1) if (g < 15) { VLD(2, sn) } SB();
      VMAC(2) if (g < 15) { VLD(4, sn) } SB();
      VMAC(3) if (g < 15) { VLD(6, sn) } SB();
    }
  }
#undef VLD
#undef VMAC
  STAMP(4);
  const int ln2 = opaque(lane), r2 = ln2 >> 4, c2 = ln2 & 15;
  const int hcol = 64 * w + 4 * c2 + 2 * (ln2 >> 5) + (r2 & 1);
  const unsigned xcol = x[b * DM + hcol];
  {
    ROWRED(acc, z0, z1)
    *reinterpret_cast<f32x2*>(lds + L_QP + (w * DH + 8 * c2 + 4 * (ln2 >> 5) + 2 * (r2 & 1)) * 4) = f32x2{z0, z1};
  }
  for (unsigned spin = 0; w == 0 && !ready && spin < SPIN_CAP; ++spin) { HS_STEP __builtin_amdgcn_s_sleep(2); }
#undef HS_STEP
#undef HS_INIT
  __syncthreads();
  STAMP(10);
  {
    const int d = (ln2 >> 3) * 16 + (ln2 & 7);
    const float* op = reinterpret_cast<const float*>(lds + L_QP) + d;
    float s0 = 0.f, s1 = 0.f;
#pragma unroll
    for (int k = 0; k < NW; ++k) { s0 += op[k * DH]; s1 += op[k * DH + 8]; }
    *reinterpret_cast<unsigned*>(lds + L_OF + w * 256 + (ln2 & 7) * 32 + (ln2 >> 3) * 4) = pk2(s0, s1);
    __builtin_amdgcn_wave_barrier();
  }
  STAMP(11);
  unsigned char* const myslot = slotb + h * SLOT + (64 * w + 8 * (ln2 & 7) + 4 * (ln2 >> 5) + 2 * (r2 & 1)) * 4;
  {
    const u32x4 ov0 = *reinterpret_cast<const u32x4*>(lds + L_OF + w * 256 + (ln2 >> 3) * 32);
    const u32x4 ov1 = *reinterpret_cast<const u32x4*>(lds + L_OF + w * 256 + (ln2 >> 3) * 32 + 16);
    float pa[8] = {0.f, 0.f, 0.f, 0.f, 0.f, 0.f, 0.f, 0.f};
#pragma unroll
    for (int e = 0; e < 16; ++e) pin(A[e]);
#pragma unroll
    for (int e = 0; e < 4; ++e) mv2p(A[2 * e], A[2 * e + 1], ov0[e], pa);
#pragma unroll
    for (int e = 0; e < 4; ++e) mv2p(A[8 + 2 * e], A[8 + 2 * e + 1], ov1[e], pa);
    ROWRED(pa, z0, z1)
    z0 += dpp<ROR8>(z0); z1 += dpp<ROR8>(z1);
    if ((ln2 & 8) == 0) st_sc1_b64(myslot + S_P1, z0, z1);
  }
  STAMP(5);

#undef LD8
  const bool on = ln2 < 8 * nch;
  const int li = on ? ln2 : 0;
  float csg = 0.f, csu = 0.f;
  {
    const float* gs = reinterpret_cast<const float*>(lds + L_GS);
#pragma unroll
    for (int k = 0; k < NW; ++k) { csg += gs[k * 96 + li]; csu += gs[k * 96 + 8 * nch + li]; }
    asm volatile("" : "+v"(csg), "+v"(csu));
  }
  STAMP(12);

  float hv;
  {
    const int off0 = r2 * SLOT + S_P1 + (64 * w + 4 * c2) * 4;
    u32x4 v[8];
    bool done[8];
    {
      const int o_ = opaque(off0);
#pragma unroll
      for (int i = 0; i < 8; ++i) v[i] = __builtin_amdgcn_raw_buffer_load_b128(rs, o_, i * 4 * SLOT, 16);
#pragma unroll
      for (int i = 0; i < 8; ++i) done[i] = __all(nosent(v[i]));
    }
    for (unsigned spin = 0; spin < SPIN_CAP; ++spin) {
      bool all = true;
#pragma unroll
      for (int i = 0; i < 8; ++i) all = all && done[i];
      if (all) break;
      __builtin_amdgcn_s_sleep(PSL);
      const int o_ = opaque(off0);
#pragma unroll
      for (int i = 0; i < 8; ++i)
        if (!done[i]) { v[i] = __builtin_amdgcn_raw_buffer_load_b128(rs, o_, i * 4 * SLOT, 16); done[i] = __all(nosent(v[i])); }
    }
    STAMP(6);
    float s[4];
#pragma unroll
    for (int k = 0; k < 4; ++k) {
      s[k] = bc<float>(v[0][k]);
#pragma unroll
      for (int i = 1; i < 8; ++i) s[k] += bc<float>(v[i][k]);
    }
    const float y0 = swapadd32(s[0], s[2]), y1 = swapadd32(s[1], s[3]);
    hv = rbf(bflo(xcol) + rbf(swapadd16(y0, y1)));
    if ((hcol >> 4) == h) reinterpret_cast<float*>(lds + L_HR)[hcol & 15] = hv;
    const unsigned hp = __builtin_amdgcn_perm(bc<unsigned>(dpp<XOR1>(hv)), bc<unsigned>(hv), 0x07060302u);
    const float mw = wave_sum(hv) * (1.f / 64.f);
    const float m2 = wave_sum((hv - mw) * (hv - mw));
    float ga[8] = {0.f, 0.f, 0.f, 0.f, 0.f, 0.f, 0.f, 0.f};
#pragma unroll
    for (int e = 0; e < 16; ++e) pin(X[e]);
#define GU(e) mv2p(X[2 * e], X[2 * e + 1], (unsigned)__builtin_amdgcn_mov_dpp((int)hp, 0x150 + 2 * e, 0xf, 0xf, true), ga);
    GU(0) GU(1) GU(2) GU(3) GU(4) GU(5) GU(6) GU(7)
#undef GU
    if (ln2 == 0) *reinterpret_cast<f32x2*>(lds + L_ST + 64 + w * 8) = f32x2{mw, m2};
    ROWRED(ga, z0, z1)
    if (c2 < 2 * nch) *reinterpret_cast<f32x2*>(lds + L_GP + (w * 96 + c2 * 8 + 4 * (ln2 >> 5) + 2 * (r2 & 1)) * 4) = f32x2{z0, z1};
  }
  STAMP(14);
  __syncthreads();
  STAMP(15);
  {
    const f32x2 st = *reinterpret_cast<const f32x2*>(lds + L_ST + 64 + (c2 & 7) * 8);
    const float* gp = reinterpret_cast<const float*>(lds + L_GP);
    float g = 0.f, u = 0.f;
#pragma unroll
    for (int k = 0; k < NW; ++k) { g += gp[k * 96 + li]; u += gp[k * 96 + 8 * nch + li]; }
    const float mean = row_sum(st[0]) * (0.5f / 8.f);
    const float var = row_sum(fmaf(64.f * (st[0] - mean), st[0] - mean, st[1])) * (0.5f / DM);
    const float inv = 1.f / rbf(sqrtf(rbf(rbf(var) + 1e-5f)));
    const float mr = rbf(mean);
    g = fmaf(-mr, csg, g) * inv; u = fmaf(-mr, csu, u) * inv;
    g = rbf(g); u = rbf(u);
    const float sg = rbf(g / (1.f + expf(-g)));
    const float a = on ? rbf(sg * u) : 0.f;
    const unsigned ap = __builtin_amdgcn_perm(bc<unsigned>(dpp<ROR8>(a)), bc<unsigned>(a), 0x07060302u);
#pragma unroll
    for (int e = 0; e < 6; ++e) pin(DW[e]);
    float da[8] = {0.f, 0.f, 0.f, 0.f, 0.f, 0.f, 0.f, 0.f};
#pragma unroll
    for (int e = 0; e < 3; ++e) {
      const unsigned xp = (unsigned)__shfl((int)ap, 16 * e + (ln2 >> 3));
      mv2p(DW[2 * e], DW[2 * e + 1], xp, da);
    }
    ROWRED(da, z0, z1)
    z0 += dpp<ROR8>(z0); z1 += dpp<ROR8>(z1);
    if ((ln2 & 8) == 0) st_sc1_b64(myslot + S_P2, z0, z1);
  }
  STAMP(7);

  if (w == 0) {
    const int off0 = (ln2 >> 2) * SLOT + S_P2 + (16 * h + 4 * (ln2 & 3)) * 4;
    u32x4 v0, v1;
    for (unsigned spin = 0;; ++spin) {
      const int o_ = opaque(off0);
      v0 = __builtin_amdgcn_raw_buffer_load_b128(rs, o_, 0, 16);
      v1 = __builtin_amdgcn_raw_buffer_load_b128(rs, o_, 16 * SLOT, 16);
      if (__all(nosent(v0) && nosent(v1)) || spin > SPIN_CAP) break;
      __builtin_amdgcn_s_sleep(1);
    }
    STAMP(8);
    float s[4];
#pragma unroll
    for (int k = 0; k < 4; ++k) s[k] = bc<float>(v0[k]) + bc<float>(v1[k]);
    const float y0 = swapadd32(s[0], s[2]), y1 = swapadd32(s[1], s[3]);
    float z = swapadd16(y0, y1);
    z += dpp<ROR8>(z); z += dpp<ROR4>(z);
    if ((ln2 & 12) == 0) {
      const int col = 4 * (ln2 & 3) + 2 * (ln2 >> 5) + (r2 & 1);
      const float hres = reinterpret_cast<const float*>(lds + L_HR)[col];
      const __bf16 o = (__bf16)(hres + rbf(z));
      out[b * DM + 16 * h + col] = bc<u16>(o);
    }
  }
  STAMP(9);
  TSFLUSH;
#undef GOFF
#undef ROWRED
}

}

extern "C" void kernel_launch(void* const* d_in, const int* in_sizes, int n_in,
                              void* d_out, int out_size, void* d_ws, size_t ws_size,
                              hipStream_t stream) {
  const u16* x = (const u16*)d_in[0];
  const u16* kc = (const u16*)d_in[1];
  const u16* vc = (const u16*)d_in[2];
  const u16* wq = (const u16*)d_in[3];
  const u16* wo = (const u16*)d_in[4];
  const u16* wg = (const u16*)d_in[6];
  const u16* wu = (const u16*)d_in[7];
  const u16* wd = (const u16*)d_in[8];
  u16* out = (u16*)d_out;
  unsigned char* ws = (unsigned char*)d_ws;
  hipLaunchKernelGGL(fused_kernel, dim3(NB * NH), dim3(NT), 0, stream, x, kc, vc, wq, wo, wg, wu, wd, out, ws);
}
```
